```python
import torch

def _softmax(x, axis=-1):
    return torch.softmax(x, dim=axis)
def _gelu(x):
    return torch.nn.functional.gelu(x, approximate="tanh")
def _silu(x):
    return torch.nn.functional.silu(x)
def _sigmoid(x):
    return torch.sigmoid(x)
def _layernorm(x, axis=-1, eps=1e-5):
    m = x.mean(dim=axis, keepdim=True)
    v = x.var(dim=axis, keepdim=True, unbiased=False)
    return (x - m) / torch.sqrt(v + eps)
def _scatter_add(tgt, idx, src, axis=0):
    return tgt.clone().index_add_(axis, idx, src)
def _segment_sum(src, seg, n):
    out = torch.zeros((n,) + src.shape[1:], dtype=src.dtype)
    return out.index_add_(0, seg, src)
def _pad(x, pw):
    flat = tuple(v for pair in reversed(pw) for v in pair)
    return torch.nn.functional.pad(x, flat)

kernel_name = 'sequential_preLN__ssm_conv_h3__dense_activation_relu__prompt'

def setup_inputs():

    _w = (lambda a: (lambda t: t.to(torch.bfloat16) if t.is_floating_point() else t)(torch.as_tensor(a)))
    def _A_setup():
        B, S, D = 1, 2048, 576
        ks = 4
        return {
            "q": _w((lambda *s: torch.randn(*s, dtype=torch.bfloat16) * 0.05)(B, S, D)),
            "k": _w((lambda *s: torch.randn(*s, dtype=torch.bfloat16) * 0.4)(B, S, D)),
            "v": _w((lambda *s: torch.randn(*s, dtype=torch.bfloat16) * 0.4)(B, S, D)),
            "shift_k": _w((lambda *s: torch.randn(*s, dtype=torch.bfloat16) * 0.4)(D, ks) * 0.5),
            "ssm_k": _w((lambda *s: torch.randn(*s, dtype=torch.bfloat16) * 0.4)(D, S) * (1.0 / S ** 0.5)),
            "ssm_D": _w((lambda *s: torch.randn(*s, dtype=torch.bfloat16) * 0.4)(D)),
        }
    _A_in = _A_setup()

    def _B_setup():
        B, S, D, Dff = 1, 2048, 576, 4608
        return {
            "x": _w((lambda *s: torch.randn(*s, dtype=torch.bfloat16) * 0.05)(B, S, D)),
            "w1": _w((lambda *s: torch.randn(*s, dtype=torch.bfloat16) * 0.05)(D, Dff)),
            "w2": _w((lambda *s: torch.randn(*s, dtype=torch.bfloat16) * 0.05)(Dff, D)),
        }
    _B_in = _B_setup()

    return {
        "A_q": _A_in["q"],
        "A_k": _A_in["k"],
        "A_v": _A_in["v"],
        "A_shift_k": _A_in["shift_k"],
        "A_ssm_k": _A_in["ssm_k"],
        "A_ssm_D": _A_in["ssm_D"],
        "B_x": _B_in["x"],
        "B_w1": _B_in["w1"],
        "B_w2": _B_in["w2"],
    }

def reference(A_q, A_k, A_v, A_shift_k, A_ssm_k, A_ssm_D, B_x, B_w1, B_w2):
    def _A_ref(q, k, v, shift_k, ssm_k, ssm_D):
        B, S, D = q.shape
        ks = shift_k.shape[-1]
        k_pad = _pad(k, ((0, 0), (ks - 1, 0), (0, 0)))
        k_shift = torch.zeros_like(k)
        for t in range(ks):
            k_shift = k_shift + k_pad[:, t:t + S, :] * shift_k[:, ks - 1 - t][None, None, :]
        kv = k_shift * v
        L = 2 * S
        kv_pad = _pad(kv, ((0, 0), (0, S), (0, 0)))
        kf_pad = _pad(ssm_k, ((0, 0), (0, S)))
        kv_f = (lambda x, axes=(-2,-1): torch.fft.rfft2(x.float(), dim=axes))(kv_pad[:, None, :, :], axes=(1, 2))
        kf_f = (lambda x, axes=(-2,-1): torch.fft.rfft2(x.float(), dim=axes))(kf_pad[:, None, :], axes=(1, 2))
        kf_f = (lambda x, *d: x.permute(*d))(kf_f, 1, 2, 0)
        y_f = kv_f * kf_f[None, :, :, :]
        y = (lambda x, s, axes=(-2,-1): torch.fft.irfft2(x, s=s, dim=axes).to(torch.bfloat16))(y_f, (1, L), axes=(1, 2))[:, 0, :S, :]
        y = y + kv * ssm_D[None, None, :]
        return q * y

    def _B_ref(x, w1, w2):
        h = torch.einsum("bsd,df->bsf", x, w1)
        zero = torch.zeros_like(h)
        h = torch.maximum(h, zero)
        return torch.einsum("bsf,fd->bsd", h, w2)

    x = A_q
    h = x + _A_ref(_layernorm(x, axis=-1), A_k, A_v, A_shift_k, A_ssm_k, A_ssm_D)
    h2 = h + _B_ref(_layernorm(h, axis=-1), B_w1, B_w2)
    return h2
```

```cpp
#include <hip/hip_runtime.h>
#include <hip/hip_bf16.h>
#include <stdint.h>

typedef unsigned short u16;
static constexpr int S = 2048, D = 576, F = 4608;

__device__ __forceinline__ float bf2f(u16 b) { return __uint_as_float(((unsigned)b) << 16); }
__device__ __forceinline__ u16 f2bf(float f) { __hip_bfloat16 h = __float2bfloat16(f); return *reinterpret_cast<u16*>(&h); }
__device__ __forceinline__ float rbf(float f) { return bf2f(f2bf(f)); }

__global__ void n_kv(const u16* k, const u16* v, const u16* sk, u16* kv) {
    int i = blockIdx.x * blockDim.x + threadIdx.x;
    if (i >= S * D) return;
    int s = i / D, d = i % D;
    float acc = 0.f;
    for (int t = 0; t < 4; ++t) {
        int ss = s - 3 + t;
        float kk = ss >= 0 ? bf2f(k[ss * D + d]) : 0.f;
        acc = rbf(acc + rbf(kk * bf2f(sk[d * 4 + 3 - t])));
    }
    kv[i] = f2bf(acc * bf2f(v[i]));
}
__global__ void n_conv(const u16* kv, const u16* kf, const u16* Dp, u16* z) {
    int i = blockIdx.x * blockDim.x + threadIdx.x;
    if (i >= S * D) return;
    int s = i / D, d = i % D;
    float acc = 0.f;
    for (int t = 0; t <= s; ++t) acc += bf2f(kv[t * D + d]) * bf2f(kf[d * S + (s - t)]);
    float y = rbf(acc);
    z[i] = f2bf(y + rbf(bf2f(kv[i]) * bf2f(Dp[d])));
}
__device__ float blk_sum(float v, float* sm) {
    for (int o = 32; o >= 1; o >>= 1) v += __shfl_xor(v, o);
    return v;
}
__global__ void n_ln(const u16* x, const u16* z, u16* h, u16* x2) {
    int row = blockIdx.x; int lane = threadIdx.x;
    float xv[9], hv[9];
    float s = 0;
    for (int i = 0; i < 9; ++i) { xv[i] = bf2f(x[row * D + lane + 64 * i]); s += xv[i]; }
    s = blk_sum(s, nullptr); float mean = s / D;
    float q = 0; for (int i = 0; i < 9; ++i) { float d = xv[i] - mean; q += d * d; }
    q = blk_sum(q, nullptr); float var = rbf(q / D); float m = rbf(mean);
    float sd = rbf(sqrtf(rbf(var + 1e-5f)));
    s = 0;
    for (int i = 0; i < 9; ++i) {
        float qn = rbf(rbf(xv[i] - m) / sd);
        float a = rbf(qn * bf2f(z[row * D + lane + 64 * i]));
        hv[i] = rbf(xv[i] + a); s += hv[i];
        h[row * D + lane + 64 * i] = f2bf(hv[i]);
    }
    s = blk_sum(s, nullptr); mean = s / D;
    q = 0; for (int i = 0; i < 9; ++i) { float d = hv[i] - mean; q += d * d; }
    q = blk_sum(q, nullptr); var = rbf(q / D); m = rbf(mean);
    sd = rbf(sqrtf(rbf(var + 1e-5f)));
    for (int i = 0; i < 9; ++i) x2[row * D + lane + 64 * i] = f2bf(rbf(hv[i] - m) / sd);
}
__global__ void n_gemm(const u16* A, const u16* B, u16* C, const u16* res, int M, int N, int K, int mode) {
    __shared__ float sa[16][17], sb[16][17];
    int tx = threadIdx.x, ty = threadIdx.y;
    int row = blockIdx.y * 16 + ty, col = blockIdx.x * 16 + tx;
    float acc = 0;
    for (int k0 = 0; k0 < K; k0 += 16) {
        sa[ty][tx] = bf2f(A[row * K + k0 + tx]);
        sb[ty][tx] = bf2f(B[(k0 + ty) * N + col]);
        __syncthreads();
        for (int k = 0; k < 16; ++k) acc += sa[ty][k] * sb[k][tx];
        __syncthreads();
    }
    if (mode == 0) C[row * N + col] = f2bf(fmaxf(rbf(acc), 0.f));
    else C[row * N + col] = f2bf(bf2f(res[row * N + col]) + rbf(acc));
}

extern "C" void kernel_launch(void* const* d_in, const int* in_sizes, int n_in,
                              void* d_out, int out_size, void* d_ws, size_t ws_size,
                              hipStream_t stream) {
    const u16* q = (const u16*)d_in[0]; const u16* k = (const u16*)d_in[1]; const u16* v = (const u16*)d_in[2];
    const u16* sk = (const u16*)d_in[3]; const u16* kf = (const u16*)d_in[4]; const u16* Dp = (const u16*)d_in[5];
    const u16* w1 = (const u16*)d_in[7]; const u16* w2 = (const u16*)d_in[8];
    u16* ws = (u16*)d_ws;
    u16* kv = ws; u16* z = kv + S * D; u16* h = z + S * D; u16* x2 = h + S * D; u16* hid = x2 + S * D;
    n_kv<<<(S * D + 255) / 256, 256, 0, stream>>>(k, v, sk, kv);
    n_conv<<<(S * D + 255) / 256, 256, 0, stream>>>(kv, kf, Dp, z);
    n_ln<<<S, 64, 0, stream>>>(q, z, h, x2);
    n_gemm<<<dim3(F / 16, S / 16), dim3(16, 16), 0, stream>>>(x2, w1, hid, nullptr, S, F, D, 0);
    n_gemm<<<dim3(D / 16, S / 16), dim3(16, 16), 0, stream>>>(hid, w2, (u16*)d_out, h, S, D, F, 1);
}
```

```cpp
#include <hip/hip_runtime.h>
#include <hip/hip_bf16.h>
#include <stdint.h>
#define SWAP
#define PRIO 1

typedef unsigned short u16;
typedef unsigned v4u __attribute__((ext_vector_type(4)));
typedef unsigned v2u __attribute__((ext_vector_type(2)));
typedef float f32x4 __attribute__((ext_vector_type(4)));
typedef float f32x2 __attribute__((ext_vector_type(2)));
typedef __bf16 bf16x2 __attribute__((ext_vector_type(2)));
typedef __bf16 bf16x8 __attribute__((ext_vector_type(8)));
typedef short s16x4 __attribute__((ext_vector_type(4)));
typedef short s16x2 __attribute__((ext_vector_type(2)));
#define LAS __attribute__((address_space(3)))
#define GAS __attribute__((address_space(1)))
#define DEVI __device__ __forceinline__
#define RLX_AGENT __ATOMIC_RELAXED, __HIP_MEMORY_SCOPE_AGENT

static constexpr int S = 2048, D = 576, F = 4608;
static constexpr int ROWB = D * 2;

static constexpr size_t OFF_CTL = 0;
static constexpr size_t OFF_ST1 = 32768;
static constexpr size_t OFF_KV = OFF_ST1 + 8192;
static constexpr size_t OFF_XT = OFF_KV + (size_t)S * D * 2;
static constexpr size_t OFF_H = OFF_XT + (size_t)S * D * 2;
static constexpr size_t OFF_W1 = OFF_H + (size_t)S * D * 2;
static constexpr size_t OFF_W2 = OFF_W1 + (size_t)32 * 172 * 1024;
static constexpr size_t OFF_HID = OFF_W2 + (size_t)D * F * 2;
static constexpr size_t OFF_IN = OFF_HID + (size_t)S * F * 2;
static constexpr int INB = 27 * 1024;
static constexpr size_t OFF_WF = OFF_IN + (size_t)256 * INB;
static constexpr size_t OFF_ST = OFF_WF + 65536;
static_assert(OFF_KV % 1024 == 0, "align");

DEVI float lo2f(unsigned u) { return __uint_as_float(u << 16); }
DEVI float hi2f(unsigned u) { return __uint_as_float(u & 0xffff0000u); }
DEVI f32x2 up2(unsigned u) { return f32x2{lo2f(u), hi2f(u)}; }
DEVI unsigned pk2(f32x2 v) { return __builtin_bit_cast(unsigned, __builtin_convertvector(v, bf16x2)); }
DEVI unsigned pk(float a, float b) { return pk2(f32x2{a, b}); }
DEVI float sum2(unsigned u, float c) { const bf16x2 one = {(__bf16)1.0f, (__bf16)1.0f}; return __builtin_amdgcn_fdot2_f32_bf16(__builtin_bit_cast(bf16x2, u), one, c, false); }
DEVI float rbf(float a) { return lo2f(pk(a, a)); }
DEVI unsigned relu2(unsigned u) { s16x2 z = {0, 0}; return __builtin_bit_cast(unsigned, __builtin_elementwise_max(__builtin_bit_cast(s16x2, u), z)); }
DEVI f32x4 mfma(v4u a, v4u b, f32x4 c) {
  return __builtin_amdgcn_mfma_f32_16x16x32_bf16(__builtin_bit_cast(bf16x8, a), __builtin_bit_cast(bf16x8, b), c, 0, 0, 0);
}
template <int N> struct IC { static constexpr int v = N; };
template <int I, int N, class Fn> DEVI void sfor(Fn&& f) { if constexpr (I < N) { f(IC<I>{}); sfor<I + 1, N>(f); } }

template <int CTRL> DEVI float dpp_add(float v) {
  return v + __builtin_bit_cast(float, __builtin_amdgcn_update_dpp(0, __builtin_bit_cast(int, v), CTRL, 0xf, 0xf, true));
}
DEVI float row16_sum(float v) { v = dpp_add<0xB1>(v); v = dpp_add<0x4E>(v); v = dpp_add<0x141>(v); return dpp_add<0x140>(v); }
DEVI float wave_sum(float v) {
  int iv = __builtin_bit_cast(int, row16_sum(v));
  float a = __builtin_bit_cast(float, __builtin_amdgcn_readlane(iv, 0)), b = __builtin_bit_cast(float, __builtin_amdgcn_readlane(iv, 16));
  float c = __builtin_bit_cast(float, __builtin_amdgcn_readlane(iv, 32)), d = __builtin_bit_cast(float, __builtin_amdgcn_readlane(iv, 48));
  return (a + b) + (c + d);
}
DEVI void glds16(const void* sbase, unsigned voff, unsigned dst) {
  unsigned keep;
  asm volatile("s_mov_b32 %0, m0\n\ts_mov_b32 m0, %3\n\ts_nop 0\n\tglobal_load_lds_dwordx4 %1, %2\n\ts_mov_b32 m0, %0"
               : "=&s"(keep) : "v"(voff), "s"(sbase), "s"(dst) : "memory");
}
DEVI void gld16(v4u& d, const void* sbase, unsigned voff) { asm volatile("global_load_dwordx4 %0, %1, %2" : "=v"(d) : "v"(voff), "s"(sbase) : "memory"); }
DEVI void gld8(v2u& d, const void* sbase, unsigned voff) { asm volatile("global_load_dwordx2 %0, %1, %2" : "=v"(d) : "v"(voff), "s"(sbase) : "memory"); }
DEVI void gld16_sc1(v4u& d, const void* sbase, unsigned voff) { asm volatile("global_load_dwordx4 %0, %1, %2 sc1" : "=v"(d) : "v"(voff), "s"(sbase) : "memory"); }
DEVI void gld8_sc1(v2u& d, const void* sbase, unsigned voff) { asm volatile("global_load_dwordx2 %0, %1, %2 sc1" : "=v"(d) : "v"(voff), "s"(sbase) : "memory"); }
DEVI void gld4_sc1(unsigned& d, const void* sbase, unsigned voff) { asm volatile("global_load_dword %0, %1, %2 sc1" : "=v"(d) : "v"(voff), "s"(sbase) : "memory"); }
DEVI void gst16_sc1(v4u d, void* sbase, unsigned voff) { asm volatile("global_store_dwordx4 %0, %1, %2 sc1\n\ts_nop 1" ::"v"(voff), "v"(d), "s"(sbase) : "memory"); }
DEVI void gst8_sc1(v2u d, void* sbase, unsigned voff) { asm volatile("global_store_dwordx2 %0, %1, %2 sc1" ::"v"(voff), "v"(d), "s"(sbase) : "memory"); }
#define BUFLD(name, T, text)                                                                                     \
  template <int IMM, class U> DEVI void name(T& d, v4u srd, unsigned voff, U&& soff) {                           \
    asm volatile("" : "+s"(soff));                                                                               \
    asm volatile(text : "=v"(d) : "v"(voff), "s"(srd), "s"(soff + IMM) : "memory");                              \
  }
#define BUFST(name, T, text)                                                                                     \
  template <int IMM, class U> DEVI void name(T d, v4u srd, unsigned voff, U&& soff) {                            \
    asm volatile("" : "+s"(soff));                                                                               \
    asm volatile(text ::"v"(d), "v"(voff), "s"(srd), "s"(soff + IMM) : "memory");                                \
  }
BUFLD(bl16, v4u, "buffer_load_dwordx4 %0, %1, %2, %3 offen")
BUFLD(bl8, v2u, "buffer_load_dwordx2 %0, %1, %2, %3 offen")
BUFLD(bl16c, v4u, "buffer_load_dwordx4 %0, %1, %2, %3 offen sc1")
BUFLD(bl8c, v2u, "buffer_load_dwordx2 %0, %1, %2, %3 offen sc1")
BUFLD(bl4c, unsigned, "buffer_load_dword %0, %1, %2, %3 offen sc1")
BUFST(bs16c, v4u, "buffer_store_dwordx4 %0, %1, %2, %3 offen sc1\n\ts_nop 1")
BUFST(bs8c, v2u, "buffer_store_dwordx2 %0, %1, %2, %3 offen sc1")
template <int IMM, int LIMM> DEVI void blds16(v4u srd, unsigned voff, unsigned& soff, unsigned& lds) {
  asm volatile("" : "+s"(soff), "+s"(lds));
  asm volatile("s_mov_b32 m0, %3\n\ts_nop 0\n\tbuffer_load_dwordx4 %0, %1, %2 offen lds" ::"v"(voff), "s"(srd), "s"(soff + IMM), "s"(lds + LIMM) : "memory");
}
template <class T> DEVI void pin(T& x) { asm volatile("" : "+v"(x)); }

#define STP_PAR 0
#define STAMP_DECL
#define ST(idx) do {} while (0)
#define STAMP_END(ws, kern) do {} while (0)

DEVI int conv_cg(int b) { return 18 * (b & 7) + (b >> 3); }

namespace sch {
constexpr int UF = 126;
#define HD __host__ __device__ constexpr
HD int p1_unit(int kl, int tau) { return tau < 4 ? 5 * kl + tau : tau == 8 ? 5 * kl + 4 : tau < 6 ? 95 + 2 * kl + (tau - 4) : 133 + 2 * kl + (tau - 6); }
HD int p2_unit(int kk, int tp) { return tp < 7 ? 7 * kk + tp : UF + 2 * kk + (tp - 7); }
HD int kpair(int p) { return p < 2 ? 8 * p : 4 + 8 * p; }
HD int kprime(int pr, int r) { return r == 4 ? 16 + pr : kpair(r % 5) + 2 * pr + r / 5; }
}

static constexpr int K0_K = 0, K0_V = 11264, K0_X = K0_V + 10240, K0_SK = K0_X + 10240, K0_O = K0_SK + 3072, K0_PF = K0_O + 72 * 144, K0_LDS = K0_PF + 9216;

__global__ __launch_bounds__(704) void k0_prep(const char* __restrict__ xg, const char* __restrict__ kg, const char* __restrict__ vg,
                                               const char* __restrict__ skg, const char* __restrict__ kfg, char* __restrict__ ws) {
  __shared__ __attribute__((aligned(16))) char sm[K0_LDS];
  asm volatile("" ::"s"(xg), "s"(kg), "s"(vg), "s"(skg), "s"(kfg), "s"(ws));
  const int a = blockIdx.x, tid = threadIdx.x, lane = tid & 63;
  const int w = __builtin_amdgcn_readfirstlane(tid >> 6);
  const unsigned lbase = (unsigned)(size_t)(LAS char*)sm;
  const int tb = a >> 1, hf = a & 1;
  STAMP_DECL;
  int row = tid / 37, col = tid - row * 37;
  col = col > 35 ? 35 : col;
  const int gr = 16 * tb - 3 + (row > 18 ? 18 : row);
  const unsigned vo = (16 * tb + (row > 15 ? 15 : row)) * ROWB + hf * 576 + col * 16;
  glds16(kg, (gr < 0 ? 0 : gr) * ROWB + hf * 576 + col * 16, lbase + K0_K + w * 1024);
  if (w < 10) glds16(vg, vo, lbase + K0_V + w * 1024);
  if (w < 3) glds16(skg, hf * 2304 + (tid < 144 ? tid : 143) * 16, lbase + K0_SK + w * 1024);
  if (w < 10) glds16(xg, vo, lbase + K0_X + w * 1024);
  if (w < 9) {
    glds16(kfg, (18 * (a & 7)) * 16384 + (a >> 3) * 9216 + tid * 16, lbase + K0_PF + w * 1024);
    asm volatile("s_waitcnt vmcnt(1)\n\ts_barrier" ::: "memory");
    ST(2);
    const int rp = tid & 7, cgl = tid >> 3;
    {
      v2u x0 = *(const v2u*)(sm + K0_X + (2 * rp) * 592 + cgl * 8), x1 = *(const v2u*)(sm + K0_X + (2 * rp + 1) * 592 + cgl * 8);
      *(v4u*)(ws + OFF_XT + (size_t)(72 * hf + cgl) * 16384 + tb * 128 + rp * 16) = v4u{x0.x, x0.y, x1.x, x1.y};
    }
    v2u kk[5], vv[2];
#pragma unroll
    for (int i = 0; i < 5; ++i) {
      v2u x = *(const v2u*)(sm + K0_K + (2 * rp + i) * 592 + cgl * 8);
      bool z = (16 * tb + 2 * rp + i) < 3;
      kk[i].x = z ? 0u : x.x; kk[i].y = z ? 0u : x.y;
    }
    vv[0] = *(const v2u*)(sm + K0_V + (2 * rp) * 592 + cgl * 8);
    vv[1] = *(const v2u*)(sm + K0_V + (2 * rp + 1) * 592 + cgl * 8);
    v4u s0 = *(const v4u*)(sm + K0_SK + cgl * 32), s1 = *(const v4u*)(sm + K0_SK + cgl * 32 + 16);
    unsigned sw[8] = {s0.x, s0.y, s0.z, s0.w, s1.x, s1.y, s1.z, s1.w};
    const int rot = (tb >> 1) & 3;
#pragma unroll
    for (int ch = 0; ch < 4; ++ch) {
      float kf[5], sk[4];
#pragma unroll
      for (int i = 0; i < 5; ++i) { unsigned u = (ch & 2) ? kk[i].y : kk[i].x; kf[i] = (ch & 1) ? hi2f(u) : lo2f(u); }
      sk[0] = lo2f(sw[2 * ch]); sk[1] = hi2f(sw[2 * ch]); sk[2] = lo2f(sw[2 * ch + 1]); sk[3] = hi2f(sw[2 * ch + 1]);
      unsigned acc = pk(kf[0] * sk[3], kf[1] * sk[3]);
#pragma unroll
      for (int j = 1; j < 4; ++j) acc = pk2(up2(acc) + up2(pk(kf[j] * sk[3 - j], kf[j + 1] * sk[3 - j])));
      unsigned u0 = (ch & 2) ? vv[0].y : vv[0].x, u1 = (ch & 2) ? vv[1].y : vv[1].x;
      float v0 = (ch & 1) ? hi2f(u0) : lo2f(u0), v1 = (ch & 1) ? hi2f(u1) : lo2f(u1);
      *(unsigned*)(sm + K0_O + cgl * 144 + (rp >> 2) * 64 + ((ch + rot) & 3) * 16 + (3 - (rp & 3)) * 4) = pk(hi2f(acc) * v1, lo2f(acc) * v0);
    }
    ST(3);
    asm volatile("" ::: "memory");
    v4u o = *(const v4u*)(sm + K0_O + cgl * 144 + rp * 16);
    *(v4u*)(ws + OFF_KV + (size_t)(72 * hf + cgl) * 16384 + tb * 128 + rp * 16) = o;
  } else {
    const int r = 8 * a + 4 * (w - 9) + (lane >> 4), p = lane & 15;
    v2u xr[9];
#pragma unroll
    for (int i = 0; i < 9; ++i) gld8(xr[i], xg, r * ROWB + (p + 16 * i) * 8);
    asm volatile("s_waitcnt vmcnt(9)\n\ts_barrier" ::: "memory");
    asm volatile("s_waitcnt vmcnt(0)" ::: "memory");
    ST(2);
#pragma unroll
    for (int i = 0; i < 9; ++i) pin(xr[i]);
    if (w == 9) {
      if (lane == 8 && a < 128) *(unsigned*)(ws + OFF_CTL + a * 256) = 0u;
      if (lane >= 16 && lane < 18) *(v4u*)(ws + OFF_WF + (a * 2 + lane - 16) * 128) = v4u{0u, 0u, 0u, 0u};
    }
    float s = 0.f, s2 = 0.f;
#pragma unroll
    for (int i = 0; i < 9; ++i) { s = sum2(xr[i].x, s); s2 = sum2(xr[i].y, s2); }
    const float mean = row16_sum(s + s2) * (1.0f / D);
    f32x2 q = {0.f, 0.f}, q2 = {0.f, 0.f};
    const f32x2 mm = {mean, mean};
#pragma unroll
    for (int i = 0; i < 9; ++i) { f32x2 d0 = up2(xr[i].x) - mm, d1 = up2(xr[i].y) - mm; q += d0 * d0; q2 += d1 * d1; }
    q += q2;
    const float var = rbf(row16_sum(q.x + q.y) * (1.0f / D));
    if (p == 0) *(unsigned*)(ws + OFF_ST1 + (((r >> 8) * 64 + (r & 63)) * 4 + ((r >> 6) & 3)) * 4) = pk(mean, sqrtf(rbf(var + 1e-5f)));
  }
  STAMP_END(ws, 0);
}

static constexpr int KF_CP = 4160, KF_CH = 4 * KF_CP;
static constexpr int L_KF = 0, L_KV = 4 * KF_CH, L_RED = L_KV + 18 * 1024, L_Z = L_RED + 32768, L_K1 = L_Z + 4 * KF_CP;

DEVI void lds_rd64(v2u& d, unsigned addr, int off) { asm volatile("ds_read_b64 %0, %1 offset:%2" : "=v"(d) : "v"(addr), "i"(off) : "memory"); }
DEVI void lds_rd128(v4u& d, unsigned addr, int off) { asm volatile("ds_read_b128 %0, %1 offset:%2" : "=v"(d) : "v"(addr), "i"(off) : "memory"); }

__global__ __launch_bounds__(512) void k1_conv(const char* __restrict__ kfg, const u16* __restrict__ ssmD,
                                               const char* __restrict__ w1g, const char* __restrict__ w2g, char* __restrict__ ws) {
  extern __shared__ __attribute__((aligned(16))) char sm[];
  asm volatile("" ::"s"(kfg), "s"(ssmD), "s"(w1g), "s"(w2g), "s"(ws));
  const int tid = threadIdx.x, lane = tid & 63;
  const int w = __builtin_amdgcn_readfirstlane(tid >> 6);
  const unsigned lbase = (unsigned)(size_t)(LAS char*)sm;
  const int g = lane >> 4, c = lane & 15;
  STAMP_DECL;
  if (blockIdx.x < 144) {
    const int cg = conv_cg(blockIdx.x), ch = w & 3, eh = w >> 2;
    const char* kfr = kfg + (size_t)(4 * cg + ch) * 4096;
    v4u kw[2];
    gld16(kw[0], kfr + eh * 2048, lane * 16);
    gld16(kw[1], kfr + eh * 2048 + 1024, lane * 16);
#pragma unroll
    for (int i = 0; i < 2; ++i) glds16(ws + OFF_KV + (size_t)cg * 16384 + (2 * w + i) * 1024, lane * 16, lbase + L_KV + (2 + 2 * w + i) * 1024);
    const v2u kmid = *(const v2u*)(kfr + 2048);
    const unsigned dpair = ((const unsigned*)ssmD)[2 * cg + (ch >> 1)];
    const float dval = (ch & 1) ? hi2f(dpair) : lo2f(dpair);
    if (tid < 128) *(v4u*)(sm + L_KV + tid * 16) = v4u{0u, 0u, 0u, 0u};
    auto put = [&](v4u a, v2u n, int X) {
      unsigned a01 = __builtin_amdgcn_alignbit(a.y, a.x, 16), a12 = __builtin_amdgcn_alignbit(a.z, a.y, 16), a23 = __builtin_amdgcn_alignbit(a.w, a.z, 16);
      unsigned a3n = __builtin_amdgcn_alignbit(n.x, a.w, 16), ann = __builtin_amdgcn_alignbit(n.y, n.x, 16);
      char* p = sm + L_KF + ch * KF_CH + X * 16;
      *(v4u*)(p) = a;
      *(v4u*)(p + KF_CP) = v4u{a01, a12, a23, a3n};
      *(v4u*)(p + 2 * KF_CP) = v4u{a.y, a.z, a.w, n.x};
      *(v4u*)(p + 3 * KF_CP) = v4u{a12, a23, a3n, ann};
    };
    asm volatile("s_waitcnt vmcnt(2)" : "+v"(kw[0]), "+v"(kw[1])::"memory");
    ST(2);
    {
      unsigned f0 = __builtin_amdgcn_readlane(kw[1].x, 0), f1 = __builtin_amdgcn_readlane(kw[1].y, 0);
      v2u n0 = {(unsigned)__builtin_amdgcn_update_dpp((int)f0, (int)kw[0].x, 0x130, 0xf, 0xf, false),
                (unsigned)__builtin_amdgcn_update_dpp((int)f1, (int)kw[0].y, 0x130, 0xf, 0xf, false)};
      v2u n1 = {(unsigned)__builtin_amdgcn_update_dpp((int)kmid.x, (int)kw[1].x, 0x130, 0xf, 0xf, false),
                (unsigned)__builtin_amdgcn_update_dpp((int)kmid.y, (int)kw[1].y, 0x130, 0xf, 0xf, false)};
      put(kw[0], n0, 128 * eh + lane + 2);
      put(kw[1], n1, 128 * eh + 64 + lane + 2);
      if (eh == 0) {
        v2u k0 = {(unsigned)__builtin_amdgcn_readlane(kw[0].x, 0), (unsigned)__builtin_amdgcn_readlane(kw[0].y, 0)};
        if (lane < 2) put(v4u{0u, 0u, 0u, 0u}, lane == 1 ? k0 : v2u{0u, 0u}, lane);
      }
    }
    ST(3);
    asm volatile("s_waitcnt vmcnt(0) lgkmcnt(0)\n\ts_barrier" ::: "memory");
    ST(5);
    const unsigned abase = lbase + L_KF + ch * KF_CH + ((1 + c) & 3) * KF_CP + (4 * (g >> 1) - 2 * (g & 1) + 2 + ((1 + c) >> 2)) * 8 + eh * 256;
    f32x4 acc[8];
#pragma unroll
    for (int i = 0; i < 8; ++i) acc[i] = f32x4{0.f, 0.f, 0.f, 0.f};
    v2u Al[2][8], Ah[2][8];
    v4u B[2][8];
    unsigned baddr[4];
#pragma unroll
    for (int e = 0; e < 4; ++e) { int x0 = 32 + 2 * c - 2 * (g >> 1) + (g & 1) - 4 * (4 * eh + e); baddr[e] = lbase + L_KV + x0 * 64 + ((ch + (x0 >> 2)) & 3) * 16; }
    auto rdA = [&](auto E, auto I) { constexpr int e = decltype(E)::v, i = decltype(I)::v; lds_rd64(Al[e & 1][i], abase, 64 * e + 512 * i); lds_rd64(Ah[e & 1][i], abase, 64 * e + 512 * i + 8); };
    auto rdB = [&](auto E, auto I) { constexpr int e = decltype(E)::v, i = decltype(I)::v; lds_rd128(B[e & 1][i], baddr[e], 2048 * i); };
    rdA(IC<0>{}, IC<0>{});
    sfor<0, 8>([&](auto I) { rdB(IC<0>{}, I); });
    sfor<1, 8>([&](auto I) { rdA(IC<0>{}, I); });
    v2u xr[4]; v4u st1;
#pragma unroll
    for (int i = 0; i < 4; ++i) gld8(xr[i], ws + OFF_XT + (size_t)cg * 16384 + (256 * w + 64 * i) * 8, lane * 8);
    gld16(st1, ws + OFF_ST1 + w * 1024, lane * 16);
    f32x2 xf[4][2], qf[4][2], df[4][2];
    sfor<0, 4>([&](auto E) {
      constexpr int e = decltype(E)::v, s = e & 1;
      if constexpr (e == 0) {
        asm volatile("s_waitcnt lgkmcnt(14)" : "+v"(Al[s][0]), "+v"(Ah[s][0]), "+v"(B[s][0]), "+v"(B[s][1]), "+v"(B[s][2]), "+v"(B[s][3]), "+v"(B[s][4]), "+v"(B[s][5]), "+v"(B[s][6]), "+v"(B[s][7])::"memory");
      } else {
        asm volatile("s_waitcnt lgkmcnt(0)"
                     : "+v"(Al[s][0]), "+v"(Al[s][1]), "+v"(Al[s][2]), "+v"(Al[s][3]), "+v"(Al[s][4]), "+v"(Al[s][5]), "+v"(Al[s][6]), "+v"(Al[s][7]),
                       "+v"(Ah[s][0]), "+v"(Ah[s][1]), "+v"(Ah[s][2]), "+v"(Ah[s][3]), "+v"(Ah[s][4]), "+v"(Ah[s][5]), "+v"(Ah[s][6]), "+v"(Ah[s][7]),
                       "+v"(B[s][0]), "+v"(B[s][1]), "+v"(B[s][2]), "+v"(B[s][3]), "+v"(B[s][4]), "+v"(B[s][5]), "+v"(B[s][6]), "+v"(B[s][7])::"memory");
      }
      __builtin_amdgcn_sched_barrier(0);
      sfor<0, 8>([&](auto AI) {
        constexpr int a = decltype(AI)::v;
        if constexpr (e == 0 && a == 1) {
          asm volatile("s_waitcnt lgkmcnt(0)"
                       : "+v"(Al[s][1]), "+v"(Al[s][2]), "+v"(Al[s][3]), "+v"(Al[s][4]), "+v"(Al[s][5]), "+v"(Al[s][6]), "+v"(Al[s][7]),
                         "+v"(Ah[s][1]), "+v"(Ah[s][2]), "+v"(Ah[s][3]), "+v"(Ah[s][4]), "+v"(Ah[s][5]), "+v"(Ah[s][6]), "+v"(Ah[s][7])::"memory");
          __builtin_amdgcn_sched_barrier(0);
        }
        v4u A = {Al[s][a].x, Al[s][a].y, Ah[s][a].x, Ah[s][a].y};
        sfor<0, 8 - a>([&](auto BI) {
          constexpr int b = decltype(BI)::v;
          acc[a + b] = mfma(A, B[s][b], acc[a + b]);
          if constexpr (e < 3 && a >= 1 && a <= 4 && b < 4) {
            if constexpr (b & 1) rdB(IC<e + 1>{}, IC<2 * (a - 1) + (b >> 1)>{});
            else rdA(IC<e + 1>{}, IC<2 * (a - 1) + (b >> 1)>{});
            __builtin_amdgcn_sched_barrier(0);
          }
        });
        __builtin_amdgcn_sched_barrier(0);
      });
      if constexpr (e == 2) {
        asm volatile("s_waitcnt vmcnt(0)" : "+v"(xr[0]), "+v"(xr[1]), "+v"(xr[2]), "+v"(xr[3]), "+v"(st1)::"memory");
        unsigned stv1[4] = {st1.x, st1.y, st1.z, st1.w};
#pragma unroll
        for (int i = 0; i < 4; ++i) {
          float m = lo2f(stv1[i]), rs = __builtin_amdgcn_rcpf(hi2f(stv1[i]));
          f32x2 mm = {m, m}, rr = {rs, rs};
          xf[i][0] = up2(xr[i].x); xf[i][1] = up2(xr[i].y);
          qf[i][0] = up2(pk2(up2(pk2(xf[i][0] - mm)) * rr));
          qf[i][1] = up2(pk2(up2(pk2(xf[i][1] - mm)) * rr));
          int x = 32 + 32 * (4 * eh + i) + 2 * c + (g >> 1);
          v2u kv = *(const v2u*)(sm + L_KV + x * 64 + ((ch + (x >> 2)) & 3) * 16 + ((g & 1) ? 0 : 8));
          df[i][0] = up2(pk(hi2f(kv.y) * dval, lo2f(kv.y) * dval));
          df[i][1] = up2(pk(hi2f(kv.x) * dval, lo2f(kv.x) * dval));
        }
        __builtin_amdgcn_sched_barrier(0);
      }
    });
    ST(9);
    f32x4 keep[4];
    if (eh) {
#pragma unroll
      for (int i = 0; i < 4; ++i) { *(f32x4*)(sm + L_RED + w * 4096 + i * 1024 + lane * 16) = acc[i]; keep[i] = acc[4 + i]; }
    } else {
#pragma unroll
      for (int i = 0; i < 4; ++i) { *(f32x4*)(sm + L_RED + w * 4096 + i * 1024 + lane * 16) = acc[4 + i]; keep[i] = acc[i]; }
    }
    asm volatile("s_waitcnt lgkmcnt(0)\n\ts_barrier" ::: "memory");
    ST(11);
#pragma unroll
    for (int i = 0; i < 4; ++i) {
      f32x4 y = keep[i] + *(const f32x4*)(sm + L_RED + (w ^ 4) * 4096 + i * 1024 + lane * 16);
      v2u z = {pk2(up2(pk(y[0], y[1])) + df[i][0]), pk2(up2(pk(y[2], y[3])) + df[i][1])};
      *(v2u*)(sm + L_Z + ch * KF_CP + (256 * (4 * eh + i) + 16 * c + 4 * g) * 2) = z;
    }
    ST(12);
    asm volatile("s_waitcnt lgkmcnt(0)\n\ts_barrier" ::: "memory");
    ST(13);
    char* hdst = ws + OFF_H + (size_t)(cg >> 3) * 1024 + ((cg >> 1) & 3) * 256 + (cg & 1) * 8;
#pragma unroll
    for (int i = 0; i < 4; ++i) {
      int t0 = 256 * w + 64 * i + 16 * g;
      s16x4 zz = __builtin_amdgcn_ds_read_tr16_b64_v4i16((LAS s16x4*)(sm + L_Z + (c >> 2) * KF_CP + (t0 + 4 * (c & 3)) * 2));
      v2u z = __builtin_bit_cast(v2u, zz);
      v2u h = {pk2(xf[i][0] + up2(pk2(qf[i][0] * up2(z.x)))), pk2(xf[i][1] + up2(pk2(qf[i][1] * up2(z.y))))};
      *(v2u*)(hdst + (t0 >> 4) * 18432 + c * 16) = h;
    }
  } else {
    const int u = (blockIdx.x - 144) * 8 + w;
    const bool isw2 = u >= 432;
    const int uu = isw2 ? u - 432 : u;
    const int kb = isw2 ? uu / 9 : uu / 72, cu = isw2 ? uu % 9 : uu % 72;
    const int ld = isw2 ? D * 2 : F * 2;
    const char* src = (isw2 ? w2g : w1g) + (size_t)kb * 96 * ld + cu * 128;
    const unsigned voff = (lane >> 3) * ld + (lane & 7) * 16;
    const char* wl = sm + w * 12288;
#pragma unroll
    for (int p = 0; p < 12; ++p) glds16(src + (size_t)p * 8 * ld, voff, lbase + w * 12288 + p * 1024);
    float cs[4] = {0.f, 0.f, 0.f, 0.f};
    const int q = c >> 2, p4 = c & 3;
    const int rlo = isw2 ? 4 * g + q : 8 * g + q, rhi = isw2 ? 16 : 4;
    int dcol[4], dtile[4];
#pragma unroll
    for (int i = 0; i < 4; ++i) {
      int blk = 2 * cu + (i >> 1), r = blk % 9, pr = blk / 9, sub = i & 1;
      bool inter = isw2 && r != 4;
      dcol[i] = (i >> 1) * 64 + (inter ? p4 * 16 + sub * 8 : sub * 32 + p4 * 8);
      int own = 2 * pr + (r > 4 || (r == 4 && sub));
      int tau = r == 4 ? 8 : 2 * (r > 4 ? r - 5 : r) + sub;
      dtile[i] = own * 16 + tau;
    }
    sfor<0, 3>([&](auto UN) {
      constexpr int un = decltype(UN)::v;
      asm volatile("s_waitcnt vmcnt(8)" ::: "memory");
      const int kst = 3 * kb + un;
#pragma unroll
      for (int i = 0; i < 4; ++i) {
        const char* p = wl + un * 4096 + rlo * 128 + dcol[i];
        v2u lo = __builtin_bit_cast(v2u, __builtin_amdgcn_ds_read_tr16_b64_v4i16((LAS s16x4*)p));
        v2u hi = __builtin_bit_cast(v2u, __builtin_amdgcn_ds_read_tr16_b64_v4i16((LAS s16x4*)(p + rhi * 128)));
        v4u f = {lo.x, lo.y, hi.x, hi.y};
        cs[i] = sum2(f.w, sum2(f.z, sum2(f.y, sum2(f.x, cs[i]))));
        const int own = dtile[i] >> 4, tau = dtile[i] & 15;
        size_t dst;
        if (!isw2) dst = OFF_W1 + (size_t)(own * 172 + sch::p1_unit(kst, tau)) * 1024;
        else {
          int s = kst / 36, kl = kst % 36, kp = sch::kprime(kl / 9, kl % 9);
          int tp = tau == 8 ? 0 : ((tau >> 1) == s ? 7 + (tau & 1) : 1 + 2 * (((tau >> 1) - s - 1) & 3) + (tau & 1));
          dst = OFF_W2 + (size_t)((s * 4 + own) * 324 + 2 * sch::p2_unit(kp >> 1, tp) + (kp & 1)) * 1024;
        }
        gst16_sc1(f, ws + dst, lane * 16);
      }
    });
    if (!isw2) {
#pragma unroll
      for (int i = 0; i < 4; ++i) {
        float v = cs[i];
        v += __shfl_xor(v, 16); v += __shfl_xor(v, 32);
        float h1 = rbf(v), h2 = rbf(v - h1);
        char* fr = ws + OFF_W1 + (size_t)((dtile[i] >> 4) * 172 + sch::p1_unit(18, dtile[i] & 15)) * 1024;
        if (lane < 16) *(v2u*)(fr + ((kb >> 1) * 16 + c) * 16 + (kb & 1) * 8) = v2u{pk(h1, h2), pk(v - h1 - h2, 0.f)};
        else if (lane < 32 && kb == 0) *(v4u*)(fr + (48 + c) * 16) = v4u{0u, 0u, 0u, 0u};
      }
    }
  }
  STAMP_END(ws, 1);
}

namespace sch {
#define LA_ 5
#define CAP_ 4
#define WST_ 2
#define STH0_ 96
#define STHD_ 9
#define PUBD_ 2
#define CHKD_ 3
constexpr int RINGP = 152, LA = LA_, PF = 3, CAP = CAP_, WST = WST_;
constexpr int N1 = 171, NU = N1 + 162, NS1 = 19, NS = 37, NR = 62;
constexpr int C_STH[9] = {0, 0, 1, 1, 1, 2, 2, 3, 3};
constexpr int s_sth(int G, int i) {
  int st = 11 - G;
  for (int k = 1; k <= i; ++k) { st += 1; int rdy = (k < 5 ? 11 : k < 7 ? 15 : 19) - G; if (st < rdy) st = rdy; }
  return st;
}
constexpr int s_pub(int c) { return s_sth(0, c == 0 ? 1 : c == 1 ? 4 : c == 2 ? 6 : 8) + PUBD_; }
constexpr int s_chk(int c) { return s_pub(c) + CHKD_; }
constexpr int S_X = NS1 + UF / 9;
constexpr int S_FL = S_X + 2;
constexpr int a2class(int i) { return i < 8 ? 0 : i < 20 ? 1 : i < 28 ? 2 : 3; }
constexpr int piece_lo(int U) { return U < N1 ? U : 172 + 2 * (U - N1); }
constexpr int piece_hi(int U) { return U < N1 ? U : 172 + 2 * (U - N1) + 1; }
constexpr int u_k(int U) { return U < 95 ? U / 5 : U < 133 ? (U - 95) / 2 : U < N1 ? (U - 133) / 2 : U < N1 + UF ? (U - N1) / 7 : (U - N1 - UF) / 2; }
constexpr int u_t(int U) { return U < 95 ? (U % 5 == 4 ? 8 : U % 5) : U < 133 ? 4 + ((U - 95) & 1) : U < N1 ? 6 + ((U - 133) & 1) : U < N1 + UF ? (U - N1) % 7 : 7 + ((U - N1 - UF) & 1); }
constexpr int kmax(int st) {
  int lo = st < NS1 ? 0 : N1, m = -1;
  for (int U = lo; U < 9 * st + 9; ++U) m = u_k(U) > m ? u_k(U) : m;
  return m > 17 ? 17 : m;
}
constexpr int need_round(int st) { int U = 9 * st + 8 + LA; return piece_hi(U > NU - 1 ? NU - 1 : U) >> 3; }
constexpr int gl_need(int r) { int st = 0; while (need_round(st) < r) ++st; return st; }
constexpr int a_need(int ph, int i) { int st = ph ? NS1 : 0; while (2 * kmax(st) + 1 < i) ++st; return st; }
enum { BAR = 0, GL, A1, A2, STH, POLL, XR, STP, HRES, FLL, VAR };
struct Op { int k, a, b, pos; };
struct Prog {
  Op pre[40]; int npre;
  Op ops[NS][24]; int nops[NS];
  int wait[NS];
  bool ok;
};
constexpr Prog build(int G) {
  Prog P{};
  P.ok = true;
  int cnt = 0, gl = -1, a1 = -1, a2 = -1;
  int glseq[NR] = {}, a1seq[36] = {}, a2seq[36] = {}, extra[NS + 8] = {};
  {
    int n = 0;
    auto pre = [&](int k, int a, int b, int wgt) { P.pre[n++] = Op{k, a, b, 0}; cnt += wgt; return cnt; };
    for (int q = 0; q < PF; ++q) {
      while (a1 < 2 * kmax(q) + 1) { ++a1; a1seq[a1] = pre(A1, a1 >> 1, a1 & 1, 1); if (q == 0 && a1 == 1) while (gl < need_round(0)) { ++gl; glseq[gl] = pre(GL, gl, 0, 1); } }
      while (gl < need_round(q)) { ++gl; glseq[gl] = pre(GL, gl, 0, 1); }
      if (q == 0) pre(BAR, 0, 0, 0);
    }
    P.npre = n;
  }
  for (int st = 0; st < NS; ++st) {
    if (gl < need_round(st) || (st < NS1 ? a1 : a2) < 2 * kmax(st) + 1) P.ok = false;
    int need = glseq[need_round(st)], s = st < NS1 ? a1seq[2 * kmax(st) + 1] : a2seq[2 * kmax(st) + 1];
    need = s > need ? s : need;
    need = extra[st] > need ? extra[st] : need;
    P.wait[st] = cnt - need > 63 ? 63 : cnt - need;
    Op ld[24] = {}; int nl = 0, fixed = 0;
    for (int i = 0; i < 9; ++i) if (s_sth(G, i) == st) fixed += WST;
    if (st == S_X) fixed += 4 * WST;
    {
      int g2 = gl, b1 = a1, b2 = a2;
      for (int budget = CAP - fixed;; --budget) {
        const int war = st == 0 ? (RINGP - 8) >> 3 : (piece_hi(9 * st - 1) + RINGP - 7) >> 3;
        const int BIG = 1000;
        int ng = (g2 < NR - 1 && g2 + 1 <= war) ? gl_need(g2 + 1) : BIG;
        int na = BIG;
        if (b1 < 35) na = a_need(0, b1 + 1);
        else if (b2 < 35 && st >= s_chk(a2class(b2 + 1))) na = a_need(1, b2 + 1);
        int m = ng < na ? ng : na;
        if (m == BIG || (budget <= 0 && m - st > 2 + G)) break;
        if (na <= ng) { if (b1 < 35) { ++b1; ld[nl++] = Op{A1, b1 >> 1, b1 & 1, 0}; } else { ++b2; ld[nl++] = Op{A2, b2 >> 1, b2 & 1, 0}; } }
        else { ++g2; ld[nl++] = Op{GL, g2, 0, 0}; }
      }
    }
    int n = 0;
    auto req = [&](int at, int seq) { if (extra[at] < seq) extra[at] = seq; };
    auto opp = [&](int pos, int k, int a, int b, int wgt) { P.ops[st][n++] = Op{k, a, b, pos}; cnt += wgt; return cnt; };
    auto op = [&](int k, int a, int b, int wgt) { return opp(G ? 9 : 0, k, a, b, wgt); };
    if (st == S_X) for (int i = 0; i < 7; ++i) opp(0, XR, (i + 1) % 7, 0, 0);
    for (int c = 0; c < 4; ++c) if (st == s_chk(c) - 1) req(s_chk(c), opp(0, POLL, c, 0, 1));
    if (st == NS - 2) req(NS - 1, opp(G ? 4 : 0, FLL, 0, 0, 1));
    if (st == S_X && G) for (int i = 0; i < 4; ++i) op(STP, i, 0, i == 3 ? 0 : 1);
    for (int il = 0; il < nl; ++il) {
      int q = op(ld[il].k, ld[il].a, ld[il].b, 1);
      if (ld[il].k == GL) glseq[++gl] = q; else if (ld[il].k == A1) a1seq[++a1] = q; else a2seq[++a2] = q;
    }
    if (st == S_X && !G) for (int i = 0; i < 4; ++i) op(STP, i, 0, i == 3 ? 0 : 1);
    for (int h = 0; h < 9; ++h) if (s_sth(G, h) == st) req(s_pub(C_STH[h]), op(STH, h, 0, 1));
    if (st == S_X - 2) { op(HRES, 0, 0, 1); op(HRES, 1, 0, 1); }
    if (st == 15) op(VAR, 0, 0, 0);
    P.nops[st] = n;
    if (n > 24) P.ok = false;
  }
  return P;
}
constexpr Prog PGS[2] = {build(0), build(1)};
static_assert(PGS[0].ok && PGS[1].ok, "schedule: an operand is needed before it is requested");
static_assert(UF % 9 == 0 && S_FL < NS - 1, "exchange must fit in the loop");
static_assert(piece_lo(9 * (S_X - 1)) % RINGP - (8 * NR) % RINGP >= 56, "exchange area overlaps live ring pieces");
constexpr int first_op(int G, int st, int i) { int n = 0; while (n < PGS[G].nops[st] && PGS[G].ops[st][n].pos < i) ++n; return n; }
}

static constexpr int L_X = ((8 * sch::NR) % sch::RINGP) * 1024;
static constexpr int L_RS = sch::RINGP * 1024;
static constexpr int L_FFN = L_RS + 512;

DEVI void spin_ge(v4u srd, unsigned soff, unsigned want) {
#pragma nounroll
  for (int spin = 0; spin < (1 << 18); ++spin) {
    unsigned v;
    asm volatile("buffer_load_dword %0, off, %1, %2 sc1\n\ts_waitcnt vmcnt(0)" : "=&v"(v) : "s"(srd), "s"(soff) : "memory");
    if ((unsigned)__builtin_amdgcn_readfirstlane(v) >= want) break;
  }
}

__global__ __launch_bounds__(512) void ffn(char* __restrict__ ws, char* __restrict__ out) {
  using namespace sch;
  extern __shared__ __attribute__((aligned(16))) char sm[];
  asm volatile("" ::"s"(ws), "s"(out));
  const int tid = threadIdx.x, lane = tid & 63;
  const int w = __builtin_amdgcn_readfirstlane(tid >> 6);
  const unsigned lbase = (unsigned)(size_t)(LAS char*)sm;
  const int g = lane >> 4, c = lane & 15;
  const int bx = blockIdx.x & 7, bi = blockIdx.x >> 3;
  const int s = bx & 3, mt8 = (bx >> 2) * 4 + (bi >> 3), jl = bi & 7, j = 8 * s + jl;
  const int mt16 = 2 * mt8 + (jl >> 2), nt = jl & 3, wm = w & 3, wk = w >> 2;
  STAMP_DECL;
  const unsigned l16 = lane * 16;
  const v4u srd = {(unsigned)(size_t)ws, (unsigned)((size_t)ws >> 32) & 0xffffu, (unsigned)OFF_ST, 0x00020000u};
#define ABL 0
  const v4u srd0 = {srd.x, srd.y, 0u, srd.w};
  const v4u srdG = (ABL & 1) ? srd0 : srd, srdA = (ABL & 2) ? srd0 : srd, srdS = (ABL & 4) ? srd0 : srd;
  unsigned w1src = OFF_W1 + j * 172 * 1024 + w * 1024;
  unsigned w2src = OFF_W2 + (s * 4 + nt) * 324 * 1024 - 172 * 1024 + w * 1024;
  unsigned a1src = OFF_H + (16 * mt8 + 2 * w) * 18 * 1024;
  unsigned a2src0 = OFF_HID + ((8 * mt16 + 2 * wm + wk) * 144 + 36 * s + wk) * 1024;
  unsigned a2src1 = OFF_HID + ((8 * mt16 + 2 * wm + 1 - wk) * 144 + 36 * s + wk) * 1024;
  const unsigned hdst = OFF_HID + ((16 * mt8 + 2 * w) * 144 + 36 * s) * 1024;
  unsigned cnt = OFF_CTL + (mt8 * 4 + s) * 256;
  const unsigned inb = OFF_IN + (mt16 * 4 + nt) * 4 * INB;
  unsigned wf = OFF_WF + ((mt16 * 4 + nt) * 8 + w) * 128;
  unsigned ldsw = lbase + w * 1024;
  const int pr = jl >> 1, odd = jl & 1;
  const bool dovar = wk == (jl >> 2);
  const int mfin = 8 * mt16 + 2 * wm + wk;
  const int pcol = 144 * nt + 16 * (nt & 1) + 32 * s;
  const int scol = 144 * nt + ((nt & 1) ? 0 : 128);

  v4u A1r[19][2], A2r[18][2], Bq[LA + 1], hres, fl, q[3];
  v2u hres1, q1[3];
  unsigned pollv[4];
  f32x4 acc[9][2], acc2[9][2], sum[2], gram[2], xr[7];
  float mean[2];
#pragma unroll
  for (int i = 0; i < 9; ++i) { acc[i][0] = acc[i][1] = acc2[i][0] = acc2[i][1] = f32x4{0.f, 0.f, 0.f, 0.f}; }
  sum[0] = sum[1] = gram[0] = gram[1] = f32x4{0.f, 0.f, 0.f, 0.f};
  const v4u ones = {0x3f803f80u, 0x3f803f80u, 0x3f803f80u, 0x3f803f80u};
  unsigned bl[3] = {lbase + l16, lbase + l16 + 65536, lbase + l16 + 131072};
  pin(bl[0]); pin(bl[1]); pin(bl[2]);

  auto half = [&](f32x4 a) -> v2u { return v2u{relu2(pk(a[0], a[1])), relu2(pk(a[2], a[3]))}; };
  auto doop = [&](auto K, auto AA, auto BB) {
    constexpr int k = decltype(K)::v, a = decltype(AA)::v, b = decltype(BB)::v;
    if constexpr (k == BAR) asm volatile("s_barrier" ::: "memory");
    if constexpr (k == GL) {
      if constexpr (a == 21) w1src = w < 4 ? w1src : w2src;
      blds16<a * 8192, ((8 * a) % RINGP) * 1024>(srdG, l16, a <= 21 ? w1src : w2src, ldsw);
    }
    if constexpr (k == A1) bl16<(b * 18 + a) * 1024>(A1r[a][b], srdA, l16, a1src);
    if constexpr (k == A2) bl16c<a * 2048>(A2r[a][b], srdA, l16, b ? a2src1 : a2src0);
    if constexpr (k == HRES) {
      if constexpr (a == 0) bl16<(int)OFF_H>(hres, srd, l16, (unsigned)((mfin * 18 + (pcol >> 5)) * 1024));
      else bl8<(int)OFF_H>(hres1, srd, (((scol >> 3) & 3) + (g >> 1)) * 256 + c * 16 + (g & 1) * 8, (unsigned)((mfin * 18 + (scol >> 5)) * 1024));
    }
    if constexpr (k == STH) {
      if constexpr (a == 4) {
        v2u x = half(acc[8][0]), y = half(acc[8][1]);
        const bool sw = pr & 1;
        bs16c<16 * 1024>(v4u{sw ? y.x : x.x, sw ? y.y : x.y, sw ? x.x : y.x, sw ? x.y : y.y}, srdS, l16, hdst + (((pr ^ odd) & 1) * 144 + pr) * 1024);
      } else {
        constexpr int i = a < 4 ? a : a - 1, m = i & 1, p = i >> 1;
        v2u x = half(acc[2 * p][m]), y = half(acc[2 * p + 1][m]);
        bs16c<(m * 144 + kpair(p)) * 1024>(v4u{x.x, x.y, y.x, y.y}, srdS, l16, hdst + jl * 1024);
      }
    }
    if constexpr (k == VAR) {
      if (dovar) {
#pragma unroll
        for (int m = 0; m < 2; ++m) {
          const int r = c & 3;
          float sq = r == 0 ? gram[m][0] : r == 1 ? gram[m][1] : r == 2 ? gram[m][2] : gram[m][3];
          float var = rbf(fmaxf(sq * (1.0f / D) - mean[m] * mean[m], 0.f));
          if ((c >> 2) == g) *(float*)(sm + L_RS + (wm * 2 + m) * 64 + c * 4) = __builtin_amdgcn_rcpf(rbf(__builtin_amdgcn_sqrtf(rbf(var + 1e-5f))));
        }
      }
    }
    if constexpr (k == POLL) bl4c<a * 8192>(pollv[a], srd, 0u, cnt);
    if constexpr (k == FLL) bl16c<0>(fl, srd, 0u, wf);
    if constexpr (k == XR) xr[a] = *(const f32x4*)(sm + L_X + (w ^ 4) * 7168 + a * 1024 + l16);
    if constexpr (k == STP) {
      if constexpr (a < 3) {
        f32x4 x = acc2[1 + 2 * a][0] + xr[1 + 2 * a], y = acc2[2 + 2 * a][0] + xr[2 + 2 * a];
        bs16c<(2 - a) * 8192>(v4u{pk(x[0], x[1]), pk(x[2], x[3]), pk(y[0], y[1]), pk(y[2], y[3])}, srdS, l16, inb + ((s + 1 + a) & 3) * INB + (2 * wm + wk) * 1024);
      } else {
        acc2[0][0] += xr[0];
        if (wm != s) bs8c<24576>(v2u{pk(acc2[0][0][0], acc2[0][0][1]), pk(acc2[0][0][2], acc2[0][0][3])}, srd, lane * 8,
                                 inb + wm * INB + ((s - wm - 1) & 3) * 1024 + wk * 512);
      }
    }
  };
  auto rdB = [&](auto UU) {
    constexpr int U = decltype(UU)::v;
    constexpr int off = (piece_lo(U) % RINGP) * 1024;
    if constexpr (U == N1) {
#pragma unroll
      for (int i = 0; i < 3; ++i) { bl[i] += wk * 1024; pin(bl[i]); }
    }
    if constexpr (U < NU) Bq[U % (LA + 1)] = *(const LAS v4u*)(size_t)(bl[off >> 16] + (off & 65535));
  };

  auto run = [&](auto GG) {
  constexpr int G = decltype(GG)::v;
  if constexpr (G == 1) __builtin_amdgcn_s_setprio(PRIO);
  sfor<0, PGS[G].npre>([&](auto I) { constexpr Op o = PGS[G].pre[decltype(I)::v]; doop(IC<o.k>{}, IC<o.a>{}, IC<o.b>{}); });
  sfor<0, NS>([&](auto SS) {
    constexpr int st = decltype(SS)::v;
    auto ops_at = [&](auto PP) {
      constexpr int p = decltype(PP)::v;
      sfor<first_op(G, st, p), first_op(G, st, p + 1)>([&](auto I) { constexpr Op o = PGS[G].ops[st][decltype(I)::v]; doop(IC<o.k>{}, IC<o.a>{}, IC<o.b>{}); });
    };
    if constexpr (st == S_FL) {
      asm volatile("s_waitcnt vmcnt(0)" ::: "memory");
      if (lane == 0) __hip_atomic_store((GAS unsigned*)(ws + wf + s * 4), 1u, RLX_AGENT);
    }
    if constexpr (st == S_X) asm volatile("s_waitcnt vmcnt(%0) lgkmcnt(0)\n\ts_barrier" ::"i"(PGS[G].wait[st]) : "memory");
    else asm volatile("s_waitcnt vmcnt(%0)\n\ts_barrier" ::"i"(PGS[G].wait[st]) : "memory");
    sfor<(st == 0 || st == NS1) ? 0 : kmax(st - 1) + 1, kmax(st) + 1>([&](auto KK) {
      constexpr int k = decltype(KK)::v;
      if constexpr (st < NS1) { pin(A1r[k][0]); pin(A1r[k][1]); }
      else {
        pin(A2r[k][0]); pin(A2r[k][1]);
        if constexpr (k == 8 || k == 9) {
          v4u x = A2r[k][0], y = A2r[k][1];
          A2r[k][0] = v4u{x.x, x.y, y.x, y.y}; A2r[k][1] = v4u{x.z, x.w, y.z, y.w};
        }
      }
    });
    sfor<0, 4>([&](auto CC) {
      constexpr int cl = decltype(CC)::v;
      if constexpr (st == s_pub(cl) && G == 0) { if ((tid & 255) == 0) __hip_atomic_fetch_add((GAS unsigned*)(ws + cnt + cl * 8192), 1u, RLX_AGENT); }
      if constexpr (st == s_chk(cl)) {
        pin(pollv[cl]);
        if ((unsigned)__builtin_amdgcn_readfirstlane(pollv[cl]) < 8u) spin_ge(srd, cnt + cl * 8192, 8u);
      }
    });
    if constexpr (st == 0) sfor<0, LA>(rdB);
    if constexpr (st == NS - 1) {
      pin(fl);
#pragma nounroll
      for (int spin = 0; spin < (1 << 18) && __builtin_amdgcn_readfirstlane(fl.x + fl.y + fl.z + fl.w) != 4; ++spin)
        asm volatile("buffer_load_dwordx4 %0, off, %1, %2 sc1\n\ts_waitcnt vmcnt(0)" : "=&v"(fl) : "s"(srd), "s"(wf) : "memory");
      ST(10);
      const unsigned myin = inb + s * INB;
      sfor<0, 3>([&](auto I) { bl16c<decltype(I)::v * 8192>(q[decltype(I)::v], srd, l16, myin + (2 * wm + wk) * 1024); });
      sfor<0, 3>([&](auto I) { bl8c<24576 + decltype(I)::v * 1024>(q1[decltype(I)::v], srd, wm == s ? lane * 8 : 0, myin + wk * 512); });
    }
    ops_at(IC<0>{});
    if constexpr (st >= 11 && st <= 14) {
      if (dovar) {
        sfor<(st - 11) * 5, (st == 14 ? 18 : (st - 10) * 5)>([&](auto KK) {
          constexpr int k = decltype(KK)::v;
          gram[0] = mfma(A1r[k][0], A1r[k][0], gram[0]);
          gram[1] = mfma(A1r[k][1], A1r[k][1], gram[1]);
        });
        pin(gram[0]); pin(gram[1]);
      }
    }
    __builtin_amdgcn_sched_barrier(0);
    sfor<0, 9>([&](auto TT) {
      constexpr int i = decltype(TT)::v, U = 9 * st + i, k = u_k(U), t = u_t(U);
      if constexpr (U == 88) {
#pragma unroll
        for (int m = 0; m < 2; ++m) { mean[m] = sum[m][0] * (1.0f / D); unsigned nm = pk(-mean[m], -mean[m]); A1r[18][m] = v4u{nm, nm, nm, nm}; }
      }
      if constexpr (st < NS1) {
        acc[t][0] = mfma(Bq[U % (LA + 1)], A1r[k][0], acc[t][0]);
        acc[t][1] = mfma(Bq[U % (LA + 1)], A1r[k][1], acc[t][1]);
        pin(acc[t][0]); pin(acc[t][1]);
        if constexpr (U < 90 && t == 0) { sum[0] = mfma(ones, A1r[k][0], sum[0]); sum[1] = mfma(ones, A1r[k][1], sum[1]); pin(sum[0]); pin(sum[1]); }
      } else {
        acc2[t][0] = mfma(Bq[U % (LA + 1)], A2r[k][0], acc2[t][0]);
        acc2[t][1] = mfma(Bq[U % (LA + 1)], A2r[k][1], acc2[t][1]);
        pin(acc2[t][0]); pin(acc2[t][1]);
      }
      rdB(IC<U + LA>{});
      if constexpr (U > N1 + UF - 7 && U < N1 + UF) *(f32x4*)(sm + L_X + w * 7168 + (t - 1) * 1024 + l16) = acc2[t - 1][1];
      if constexpr (U == N1 + UF - 1) *(f32x4*)(sm + L_X + w * 7168 + t * 1024 + l16) = acc2[t][1];
      ops_at(IC<i + 1>{});
      __builtin_amdgcn_sched_barrier(0);
    });
  });
  ST(11);
  *(f32x4*)(sm + L_X + w * 7168 + l16) = acc2[7][1];
  *(f32x4*)(sm + L_X + w * 7168 + 1024 + l16) = acc2[8][1];
  asm volatile("s_waitcnt lgkmcnt(0)\n\ts_barrier" ::: "memory");
  const f32x4 o0 = acc2[7][0] + *(const f32x4*)(sm + L_X + (w ^ 4) * 7168 + l16);
  const f32x4 o1 = acc2[8][0] + *(const f32x4*)(sm + L_X + (w ^ 4) * 7168 + 1024 + l16);
  const float rs = *(const float*)(sm + L_RS + (2 * wm + wk) * 64 + c * 4);
  ST(12);
  asm volatile("s_waitcnt vmcnt(0)" : "+v"(q[0]), "+v"(q[1]), "+v"(q[2]), "+v"(q1[0]), "+v"(q1[1]), "+v"(q1[2]), "+v"(hres), "+v"(hres1)::"memory");
  ST(13);
  const f32x2 rr = {rs, rs};
  {
    f32x2 a = (f32x2{o0[0], o0[1]} + up2(q[1].x)) + (up2(q[0].x) + up2(q[2].x));
    f32x2 b = (f32x2{o0[2], o0[3]} + up2(q[1].y)) + (up2(q[0].y) + up2(q[2].y));
    f32x2 cc = (f32x2{o1[0], o1[1]} + up2(q[1].z)) + (up2(q[0].z) + up2(q[2].z));
    f32x2 d = (f32x2{o1[2], o1[3]} + up2(q[1].w)) + (up2(q[0].w) + up2(q[2].w));
    v4u r = {pk2(up2(hres.x) + up2(pk2(a * rr))), pk2(up2(hres.y) + up2(pk2(b * rr))), pk2(up2(hres.z) + up2(pk2(cc * rr))), pk2(up2(hres.w) + up2(pk2(d * rr)))};
    *(v4u*)(out + (size_t)(mfin * 16 + c) * ROWB + (pcol + 8 * g) * 2) = r;
  }
  if (wm == s) {
    f32x2 a = (f32x2{acc2[0][0][0], acc2[0][0][1]} + up2(q1[1].x)) + (up2(q1[0].x) + up2(q1[2].x));
    f32x2 b = (f32x2{acc2[0][0][2], acc2[0][0][3]} + up2(q1[1].y)) + (up2(q1[0].y) + up2(q1[2].y));
    v2u r = {pk2(up2(hres1.x) + up2(pk2(a * rr))), pk2(up2(hres1.y) + up2(pk2(b * rr)))};
    *(v2u*)(out + (size_t)(mfin * 16 + c) * ROWB + (scol + 4 * g) * 2) = r;
  }
  STAMP_END(ws, 2);
  };
  if (wk) run(IC<0>{}); else run(IC<1>{});
}

extern "C" void kernel_launch(void* const* d_in, const int* in_sizes, int n_in, void* d_out, int out_size, void* d_ws, size_t ws_size,
                              hipStream_t stream) {
  char* ws = (char*)d_ws;
  (void)hipFuncSetAttribute((const void*)k1_conv, hipFuncAttributeMaxDynamicSharedMemorySize, L_K1);
  (void)hipFuncSetAttribute((const void*)ffn, hipFuncAttributeMaxDynamicSharedMemorySize, L_FFN);
  k0_prep<<<256, 704, 0, stream>>>((const char*)d_in[0], (const char*)d_in[1], (const char*)d_in[2], (const char*)d_in[3], (const char*)d_in[4], ws);
  k1_conv<<<252, 512, L_K1, stream>>>((const char*)d_in[4], (const u16*)d_in[5], (const char*)d_in[7], (const char*)d_in[8], ws);
  ffn<<<256, 512, L_FFN, stream>>>(ws, (char*)d_out);
}
```

```cpp
#include <hip/hip_runtime.h>
#include <hip/hip_bf16.h>
#include <stdint.h>
#define SWAP
#define PRIO 1

typedef unsigned short u16;
typedef unsigned v4u __attribute__((ext_vector_type(4)));
typedef unsigned v2u __attribute__((ext_vector_type(2)));
typedef float f32x4 __attribute__((ext_vector_type(4)));
typedef float f32x2 __attribute__((ext_vector_type(2)));
typedef __bf16 bf16x2 __attribute__((ext_vector_type(2)));
typedef __bf16 bf16x8 __attribute__((ext_vector_type(8)));
typedef short s16x4 __attribute__((ext_vector_type(4)));
typedef short s16x2 __attribute__((ext_vector_type(2)));
#define LAS __attribute__((address_space(3)))
#define GAS __attribute__((address_space(1)))
#define DEVI __device__ __forceinline__
#define RLX_AGENT __ATOMIC_RELAXED, __HIP_MEMORY_SCOPE_AGENT

static constexpr int S = 2048, D = 576, F = 4608;
static constexpr int ROWB = D * 2;

static constexpr size_t OFF_CTL = 0;
static constexpr size_t OFF_ST1 = 32768;
static constexpr size_t OFF_KV = OFF_ST1 + 8192;
static constexpr size_t OFF_XT = OFF_KV + (size_t)S * D * 2;
static constexpr size_t OFF_H = OFF_XT + (size_t)S * D * 2;
static constexpr size_t OFF_W1 = OFF_H + (size_t)S * D * 2;
static constexpr size_t OFF_W2 = OFF_W1 + (size_t)32 * 172 * 1024;
static constexpr size_t OFF_HID = OFF_W2 + (size_t)D * F * 2;
static constexpr size_t OFF_IN = OFF_HID + (size_t)S * F * 2;
static constexpr int INB = 27 * 1024;
static constexpr size_t OFF_WF = OFF_IN + (size_t)256 * INB;
static constexpr size_t OFF_ST = OFF_WF + 65536;
static_assert(OFF_KV % 1024 == 0, "align");

DEVI float lo2f(unsigned u) { return __uint_as_float(u << 16); }
DEVI float hi2f(unsigned u) { return __uint_as_float(u & 0xffff0000u); }
DEVI f32x2 up2(unsigned u) { return f32x2{lo2f(u), hi2f(u)}; }
DEVI unsigned pk2(f32x2 v) { return __builtin_bit_cast(unsigned, __builtin_convertvector(v, bf16x2)); }
DEVI unsigned pk(float a, float b) { return pk2(f32x2{a, b}); }
DEVI float sum2(unsigned u, float c) { const bf16x2 one = {(__bf16)1.0f, (__bf16)1.0f}; return __builtin_amdgcn_fdot2_f32_bf16(__builtin_bit_cast(bf16x2, u), one, c, false); }
DEVI float rbf(float a) { return lo2f(pk(a, a)); }
DEVI unsigned relu2(unsigned u) { s16x2 z = {0, 0}; return __builtin_bit_cast(unsigned, __builtin_elementwise_max(__builtin_bit_cast(s16x2, u), z)); }
DEVI f32x4 mfma(v4u a, v4u b, f32x4 c) {
  return __builtin_amdgcn_mfma_f32_16x16x32_bf16(__builtin_bit_cast(bf16x8, a), __builtin_bit_cast(bf16x8, b), c, 0, 0, 0);
}
template <int N> struct IC { static constexpr int v = N; };
template <int I, int N, class Fn> DEVI void sfor(Fn&& f) { if constexpr (I < N) { f(IC<I>{}); sfor<I + 1, N>(f); } }

template <int CTRL> DEVI float dpp_add(float v) {
  return v + __builtin_bit_cast(float, __builtin_amdgcn_update_dpp(0, __builtin_bit_cast(int, v), CTRL, 0xf, 0xf, true));
}
DEVI float row16_sum(float v) { v = dpp_add<0xB1>(v); v = dpp_add<0x4E>(v); v = dpp_add<0x141>(v); return dpp_add<0x140>(v); }
DEVI float wave_sum(float v) {
  int iv = __builtin_bit_cast(int, row16_sum(v));
  float a = __builtin_bit_cast(float, __builtin_amdgcn_readlane(iv, 0)), b = __builtin_bit_cast(float, __builtin_amdgcn_readlane(iv, 16));
  float c = __builtin_bit_cast(float, __builtin_amdgcn_readlane(iv, 32)), d = __builtin_bit_cast(float, __builtin_amdgcn_readlane(iv, 48));
  return (a + b) + (c + d);
}
DEVI void glds16(const void* sbase, unsigned voff, unsigned dst) {
  unsigned keep;
  asm volatile("s_mov_b32 %0, m0\n\ts_mov_b32 m0, %3\n\ts_nop 0\n\tglobal_load_lds_dwordx4 %1, %2\n\ts_mov_b32 m0, %0"
               : "=&s"(keep) : "v"(voff), "s"(sbase), "s"(dst) : "memory");
}
DEVI void gld16(v4u& d, const void* sbase, unsigned voff) { asm volatile("global_load_dwordx4 %0, %1, %2" : "=v"(d) : "v"(voff), "s"(sbase) : "memory"); }
DEVI void gld8(v2u& d, const void* sbase, unsigned voff) { asm volatile("global_load_dwordx2 %0, %1, %2" : "=v"(d) : "v"(voff), "s"(sbase) : "memory"); }
DEVI void gld16_sc1(v4u& d, const void* sbase, unsigned voff) { asm volatile("global_load_dwordx4 %0, %1, %2 sc1" : "=v"(d) : "v"(voff), "s"(sbase) : "memory"); }
DEVI void gld8_sc1(v2u& d, const void* sbase, unsigned voff) { asm volatile("global_load_dwordx2 %0, %1, %2 sc1" : "=v"(d) : "v"(voff), "s"(sbase) : "memory"); }
DEVI void gld4_sc1(unsigned& d, const void* sbase, unsigned voff) { asm volatile("global_load_dword %0, %1, %2 sc1" : "=v"(d) : "v"(voff), "s"(sbase) : "memory"); }
DEVI void gst16_sc1(v4u d, void* sbase, unsigned voff) { asm volatile("global_store_dwordx4 %0, %1, %2 sc1\n\ts_nop 1" ::"v"(voff), "v"(d), "s"(sbase) : "memory"); }
DEVI void gst8_sc1(v2u d, void* sbase, unsigned voff) { asm volatile("global_store_dwordx2 %0, %1, %2 sc1" ::"v"(voff), "v"(d), "s"(sbase) : "memory"); }
#define BUFLD(name, T, text)                                                                                     \
  template <int IMM, class U> DEVI void name(T& d, v4u srd, unsigned voff, U&& soff) {                           \
    asm volatile("" : "+s"(soff));                                                                               \
    asm volatile(text : "=v"(d) : "v"(voff), "s"(srd), "s"(soff + IMM) : "memory");                              \
  }
#define BUFST(name, T, text)                                                                                     \
  template <int IMM, class U> DEVI void name(T d, v4u srd, unsigned voff, U&& soff) {                            \
    asm volatile("" : "+s"(soff));                                                                               \
    asm volatile(text ::"v"(d), "v"(voff), "s"(srd), "s"(soff + IMM) : "memory");                                \
  }
BUFLD(bl16, v4u, "buffer_load_dwordx4 %0, %1, %2, %3 offen")
BUFLD(bl8, v2u, "buffer_load_dwordx2 %0, %1, %2, %3 offen")
BUFLD(bl16c, v4u, "buffer_load_dwordx4 %0, %1, %2, %3 offen sc1")
BUFLD(bl8c, v2u, "buffer_load_dwordx2 %0, %1, %2, %3 offen sc1")
BUFLD(bl4c, unsigned, "buffer_load_dword %0, %1, %2, %3 offen sc1")
BUFST(bs16c, v4u, "buffer_store_dwordx4 %0, %1, %2, %3 offen sc1\n\ts_nop 1")
BUFST(bs8c, v2u, "buffer_store_dwordx2 %0, %1, %2, %3 offen sc1")
template <int IMM, int LIMM> DEVI void blds16(v4u srd, unsigned voff, unsigned& soff, unsigned& lds) {
  asm volatile("" : "+s"(soff), "+s"(lds));
  asm volatile("s_mov_b32 m0, %3\n\ts_nop 0\n\tbuffer_load_dwordx4 %0, %1, %2 offen lds" ::"v"(voff), "s"(srd), "s"(soff + IMM), "s"(lds + LIMM) : "memory");
}
template <class T> DEVI void pin(T& x) { asm volatile("" : "+v"(x)); }

#define STP_PAR 0
#define STAMP_DECL
#define ST(idx) do {} while (0)
#define STAMP_END(ws, kern) do {} while (0)

DEVI int conv_cg(int b) { return 18 * (b & 7) + (b >> 3); }

namespace sch {
constexpr int UF = 126;
#define HD __host__ __device__ constexpr
HD int p1_unit(int kl, int tau) { return tau < 4 ? 5 * kl + tau : tau == 8 ? 5 * kl + 4 : tau < 6 ? 95 + 2 * kl + (tau - 4) : 133 + 2 * kl + (tau - 6); }
HD int p2_unit(int kk, int tp) { return tp < 7 ? 7 * kk + tp : UF + 2 * kk + (tp - 7); }
HD int kpair(int p) { return p < 2 ? 8 * p : 4 + 8 * p; }
HD int kprime(int pr, int r) { return r == 4 ? 16 + pr : kpair(r % 5) + 2 * pr + r / 5; }
}

static constexpr int KF_CP = 4160, KF_CH = 4 * KF_CP;
static constexpr int L_KF = 0, L_KV = 4 * KF_CH, L_RED = L_KV + 18 * 1024, L_Z = L_RED + 32768, L_K1 = L_Z + 4 * KF_CP;

DEVI void lds_rd64(v2u& d, unsigned addr, int off) { asm volatile("ds_read_b64 %0, %1 offset:%2" : "=v"(d) : "v"(addr), "i"(off) : "memory"); }
DEVI void lds_rd128(v4u& d, unsigned addr, int off) { asm volatile("ds_read_b128 %0, %1 offset:%2" : "=v"(d) : "v"(addr), "i"(off) : "memory"); }

extern "C" __global__ __launch_bounds__(512) void k1_conv(const char* __restrict__ kfg, const u16* __restrict__ ssmD,
                                               const char* __restrict__ w1g, const char* __restrict__ w2g, char* __restrict__ ws) {
  extern __shared__ __attribute__((aligned(16))) char sm[];
  asm volatile("" ::"s"(kfg), "s"(ssmD), "s"(w1g), "s"(w2g), "s"(ws));
  const int tid = threadIdx.x, lane = tid & 63;
  const int w = __builtin_amdgcn_readfirstlane(tid >> 6);
  const unsigned lbase = (unsigned)(size_t)(LAS char*)sm;
  const int g = lane >> 4, c = lane & 15;
  STAMP_DECL;
  if (blockIdx.x < 144) {
    const int cg = conv_cg(blockIdx.x), ch = w & 3, eh = w >> 2;
    const char* kfr = kfg + (size_t)(4 * cg + ch) * 4096;
    v4u kw[2];
    gld16(kw[0], kfr + eh * 2048, lane * 16);
    gld16(kw[1], kfr + eh * 2048 + 1024, lane * 16);
#pragma unroll
    for (int i = 0; i < 2; ++i) glds16(ws + OFF_KV + (size_t)cg * 16384 + (2 * w + i) * 1024, lane * 16, lbase + L_KV + (2 + 2 * w + i) * 1024);
    const v2u kmid = *(const v2u*)(kfr + 2048);
    const unsigned dpair = ((const unsigned*)ssmD)[2 * cg + (ch >> 1)];
    const float dval = (ch & 1) ? hi2f(dpair) : lo2f(dpair);
    if (tid < 128) *(v4u*)(sm + L_KV + tid * 16) = v4u{0u, 0u, 0u, 0u};
    auto put = [&](v4u a, v2u n, int X) {
      unsigned a01 = __builtin_amdgcn_alignbit(a.y, a.x, 16), a12 = __builtin_amdgcn_alignbit(a.z, a.y, 16), a23 = __builtin_amdgcn_alignbit(a.w, a.z, 16);
      unsigned a3n = __builtin_amdgcn_alignbit(n.x, a.w, 16), ann = __builtin_amdgcn_alignbit(n.y, n.x, 16);
      char* p = sm + L_KF + ch * KF_CH + X * 16;
      *(v4u*)(p) = a;
      *(v4u*)(p + KF_CP) = v4u{a01, a12, a23, a3n};
      *(v4u*)(p + 2 * KF_CP) = v4u{a.y, a.z, a.w, n.x};
      *(v4u*)(p + 3 * KF_CP) = v4u{a12, a23, a3n, ann};
    };
    asm volatile("s_waitcnt vmcnt(2)" : "+v"(kw[0]), "+v"(kw[1])::"memory");
    ST(2);
    {
      unsigned f0 = __builtin_amdgcn_readlane(kw[1].x, 0), f1 = __builtin_amdgcn_readlane(kw[1].y, 0);
      v2u n0 = {(unsigned)__builtin_amdgcn_update_dpp((int)f0, (int)kw[0].x, 0x130, 0xf, 0xf, false),
                (unsigned)__builtin_amdgcn_update_dpp((int)f1, (int)kw[0].y, 0x130, 0xf, 0xf, false)};
      v2u n1 = {(unsigned)__builtin_amdgcn_update_dpp((int)kmid.x, (int)kw[1].x, 0x130, 0xf, 0xf, false),
                (unsigned)__builtin_amdgcn_update_dpp((int)kmid.y, (int)kw[1].y, 0x130, 0xf, 0xf, false)};
      put(kw[0], n0, 128 * eh + lane + 2);
      put(kw[1], n1, 128 * eh + 64 + lane + 2);
      if (eh == 0) {
        v2u k0 = {(unsigned)__builtin_amdgcn_readlane(kw[0].x, 0), (unsigned)__builtin_amdgcn_readlane(kw[0].y, 0)};
        if (lane < 2) put(v4u{0u, 0u, 0u, 0u}, lane == 1 ? k0 : v2u{0u, 0u}, lane);
      }
    }
    ST(3);
    asm volatile("s_waitcnt vmcnt(0) lgkmcnt(0)\n\ts_barrier" ::: "memory");
    ST(5);
    const unsigned abase = lbase + L_KF + ch * KF_CH + ((1 + c) & 3) * KF_CP + (4 * (g >> 1) - 2 * (g & 1) + 2 + ((1 + c) >> 2)) * 8 + eh * 256;
    f32x4 acc[8];
#pragma unroll
    for (int i = 0; i < 8; ++i) acc[i] = f32x4{0.f, 0.f, 0.f, 0.f};
    v2u Al[2][8], Ah[2][8];
    v4u B[2][8];
    unsigned baddr[4];
#pragma unroll
    for (int e = 0; e < 4; ++e) { int x0 = 32 + 2 * c - 2 * (g >> 1) + (g & 1) - 4 * (4 * eh + e); baddr[e] = lbase + L_KV + x0 * 64 + ((ch + (x0 >> 2)) & 3) * 16; }
    auto rdA = [&](auto E, auto I) { constexpr int e = decltype(E)::v, i = decltype(I)::v; lds_rd64(Al[e & 1][i], abase, 64 * e + 512 * i); lds_rd64(Ah[e & 1][i], abase, 64 * e + 512 * i + 8); };
    auto rdB = [&](auto E, auto I) { constexpr int e = decltype(E)::v, i = decltype(I)::v; lds_rd128(B[e & 1][i], baddr[e], 2048 * i); };
    rdA(IC<0>{}, IC<0>{});
    sfor<0, 8>([&](auto I) { rdB(IC<0>{}, I); });
    sfor<1, 8>([&](auto I) { rdA(IC<0>{}, I); });
    v2u xr[4]; v4u st1;
#pragma unroll
    for (int i = 0; i < 4; ++i) gld8(xr[i], ws + OFF_XT + (size_t)cg * 16384 + (256 * w + 64 * i) * 8, lane * 8);
    gld16(st1, ws + OFF_ST1 + w * 1024, lane * 16);
    f32x2 xf[4][2], qf[4][2], df[4][2];
    sfor<0, 4>([&](auto E) {
      constexpr int e = decltype(E)::v, s = e & 1;
      if constexpr (e == 0) {
        asm volatile("s_waitcnt lgkmcnt(14)" : "+v"(Al[s][0]), "+v"(Ah[s][0]), "+v"(B[s][0]), "+v"(B[s][1]), "+v"(B[s][2]), "+v"(B[s][3]), "+v"(B[s][4]), "+v"(B[s][5]), "+v"(B[s][6]), "+v"(B[s][7])::"memory");
      } else {
        asm volatile("s_waitcnt lgkmcnt(0)"
                     : "+v"(Al[s][0]), "+v"(Al[s][1]), "+v"(Al[s][2]), "+v"(Al[s][3]), "+v"(Al[s][4]), "+v"(Al[s][5]), "+v"(Al[s][6]), "+v"(Al[s][7]),
                       "+v"(Ah[s][0]), "+v"(Ah[s][1]), "+v"(Ah[s][2]), "+v"(Ah[s][3]), "+v"(Ah[s][4]), "+v"(Ah[s][5]), "+v"(Ah[s][6]), "+v"(Ah[s][7]),
                       "+v"(B[s][0]), "+v"(B[s][1]), "+v"(B[s][2]), "+v"(B[s][3]), "+v"(B[s][4]), "+v"(B[s][5]), "+v"(B[s][6]), "+v"(B[s][7])::"memory");
      }
      __builtin_amdgcn_sched_barrier(0);
      sfor<0, 8>([&](auto AI) {
        constexpr int a = decltype(AI)::v;
        if constexpr (e == 0 && a == 1) {
          asm volatile("s_waitcnt lgkmcnt(0)"
                       : "+v"(Al[s][1]), "+v"(Al[s][2]), "+v"(Al[s][3]), "+v"(Al[s][4]), "+v"(Al[s][5]), "+v"(Al[s][6]), "+v"(Al[s][7]),
                         "+v"(Ah[s][1]), "+v"(Ah[s][2]), "+v"(Ah[s][3]), "+v"(Ah[s][4]), "+v"(Ah[s][5]), "+v"(Ah[s][6]), "+v"(Ah[s][7])::"memory");
          __builtin_amdgcn_sched_barrier(0);
        }
        v4u A = {Al[s][a].x, Al[s][a].y, Ah[s][a].x, Ah[s][a].y};
        sfor<0, 8 - a>([&](auto BI) {
          constexpr int b = decltype(BI)::v;
          acc[a + b] = mfma(A, B[s][b], acc[a + b]);
          if constexpr (e < 3 && a >= 1 && a <= 4 && b < 4) {
            if constexpr (b & 1) rdB(IC<e + 1>{}, IC<2 * (a - 1) + (b >> 1)>{});
            else rdA(IC<e + 1>{}, IC<2 * (a - 1) + (b >> 1)>{});
            __builtin_amdgcn_sched_barrier(0);
          }
        });
        __builtin_amdgcn_sched_barrier(0);
      });
      if constexpr (e == 2) {
        asm volatile("s_waitcnt vmcnt(0)" : "+v"(xr[0]), "+v"(xr[1]), "+v"(xr[2]), "+v"(xr[3]), "+v"(st1)::"memory");
        unsigned stv1[4] = {st1.x, st1.y, st1.z, st1.w};
#pragma unroll
        for (int i = 0; i < 4; ++i) {
          float m = lo2f(stv1[i]), rs = __builtin_amdgcn_rcpf(hi2f(stv1[i]));
          f32x2 mm = {m, m}, rr = {rs, rs};
          xf[i][0] = up2(xr[i].x); xf[i][1] = up2(xr[i].y);
          qf[i][0] = up2(pk2(up2(pk2(xf[i][0] - mm)) * rr));
          qf[i][1] = up2(pk2(up2(pk2(xf[i][1] - mm)) * rr));
          int x = 32 + 32 * (4 * eh + i) + 2 * c + (g >> 1);
          v2u kv = *(const v2u*)(sm + L_KV + x * 64 + ((ch + (x >> 2)) & 3) * 16 + ((g & 1) ? 0 : 8));
          df[i][0] = up2(pk(hi2f(kv.y) * dval, lo2f(kv.y) * dval));
          df[i][1] = up2(pk(hi2f(kv.x) * dval, lo2f(kv.x) * dval));
        }
        __builtin_amdgcn_sched_barrier(0);
      }
    });
    ST(9);
    f32x4 keep[4];
    if (eh) {
#pragma unroll
      for (int i = 0; i < 4; ++i) { *(f32x4*)(sm + L_RED + w * 4096 + i * 1024 + lane * 16) = acc[i]; keep[i] = acc[4 + i]; }
    } else {
#pragma unroll
      for (int i = 0; i < 4; ++i) { *(f32x4*)(sm + L_RED + w * 4096 + i * 1024 + lane * 16) = acc[4 + i]; keep[i] = acc[i]; }
    }
    asm volatile("s_waitcnt lgkmcnt(0)\n\ts_barrier" ::: "memory");
    ST(11);
#pragma unroll
    for (int i = 0; i < 4; ++i) {
      f32x4 y = keep[i] + *(const f32x4*)(sm + L_RED + (w ^ 4) * 4096 + i * 1024 + lane * 16);
      v2u z = {pk2(up2(pk(y[0], y[1])) + df[i][0]), pk2(up2(pk(y[2], y[3])) + df[i][1])};
      *(v2u*)(sm + L_Z + ch * KF_CP + (256 * (4 * eh + i) + 16 * c + 4 * g) * 2) = z;
    }
    ST(12);
    asm volatile("s_waitcnt lgkmcnt(0)\n\ts_barrier" ::: "memory");
    ST(13);
    char* hdst = ws + OFF_H + (size_t)(cg >> 3) * 1024 + ((cg >> 1) & 3) * 256 + (cg & 1) * 8;
#pragma unroll
    for (int i = 0; i < 4; ++i) {
      int t0 = 256 * w + 64 * i + 16 * g;
      s16x4 zz = __builtin_amdgcn_ds_read_tr16_b64_v4i16((LAS s16x4*)(sm + L_Z + (c >> 2) * KF_CP + (t0 + 4 * (c & 3)) * 2));
      v2u z = __builtin_bit_cast(v2u, zz);
      v2u h = {pk2(xf[i][0] + up2(pk2(qf[i][0] * up2(z.x)))), pk2(xf[i][1] + up2(pk2(qf[i][1] * up2(z.y))))};
      *(v2u*)(hdst + (t0 >> 4) * 18432 + c * 16) = h;
    }
  } else {
    const int u = (blockIdx.x - 144) * 8 + w;
    const bool isw2 = u >= 432;
    const int uu = isw2 ? u - 432 : u;
    const int kb = isw2 ? uu / 9 : uu / 72, cu = isw2 ? uu % 9 : uu % 72;
    const int ld = isw2 ? D * 2 : F * 2;
    const char* src = (isw2 ? w2g : w1g) + (size_t)kb * 96 * ld + cu * 128;
    const unsigned voff = (lane >> 3) * ld + (lane & 7) * 16;
    const char* wl = sm + w * 12288;
#pragma unroll
    for (int p = 0; p < 12; ++p) glds16(src + (size_t)p * 8 * ld, voff, lbase + w * 12288 + p * 1024);
    float cs[4] = {0.f, 0.f, 0.f, 0.f};
    const int q = c >> 2, p4 = c & 3;
    const int rlo = isw2 ? 4 * g + q : 8 * g + q, rhi = isw2 ? 16 : 4;
    int dcol[4], dtile[4];
#pragma unroll
    for (int i = 0; i < 4; ++i) {
      int blk = 2 * cu + (i >> 1), r = blk % 9, pr = blk / 9, sub = i & 1;
      bool inter = isw2 && r != 4;
      dcol[i] = (i >> 1) * 64 + (inter ? p4 * 16 + sub * 8 : sub * 32 + p4 * 8);
      int own = 2 * pr + (r > 4 || (r == 4 && sub));
      int tau = r == 4 ? 8 : 2 * (r > 4 ? r - 5 : r) + sub;
      dtile[i] = own * 16 + tau;
    }
    sfor<0, 3>([&](auto UN) {
      constexpr int un = decltype(UN)::v;
      asm volatile("s_waitcnt vmcnt(8)" ::: "memory");
      const int kst = 3 * kb + un;
#pragma unroll
      for (int i = 0; i < 4; ++i) {
        const char* p = wl + un * 4096 + rlo * 128 + dcol[i];
        v2u lo = __builtin_bit_cast(v2u, __builtin_amdgcn_ds_read_tr16_b64_v4i16((LAS s16x4*)p));
        v2u hi = __builtin_bit_cast(v2u, __builtin_amdgcn_ds_read_tr16_b64_v4i16((LAS s16x4*)(p + rhi * 128)));
        v4u f = {lo.x, lo.y, hi.x, hi.y};
        cs[i] = sum2(f.w, sum2(f.z, sum2(f.y, sum2(f.x, cs[i]))));
        const int own = dtile[i] >> 4, tau = dtile[i] & 15;
        size_t dst;
        if (!isw2) dst = OFF_W1 + (size_t)(own * 172 + sch::p1_unit(kst, tau)) * 1024;
        else {
          int s = kst / 36, kl = kst % 36, kp = sch::kprime(kl / 9, kl % 9);
          int tp = tau == 8 ? 0 : ((tau >> 1) == s ? 7 + (tau & 1) : 1 + 2 * (((tau >> 1) - s - 1) & 3) + (tau & 1));
          dst = OFF_W2 + (size_t)((s * 4 + own) * 324 + 2 * sch::p2_unit(kp >> 1, tp) + (kp & 1)) * 1024;
        }
        gst16_sc1(f, ws + dst, lane * 16);
      }
    });
    if (!isw2) {
#pragma unroll
      for (int i = 0; i < 4; ++i) {
        float v = cs[i];
        v += __shfl_xor(v, 16); v += __shfl_xor(v, 32);
        float h1 = rbf(v), h2 = rbf(v - h1);
        char* fr = ws + OFF_W1 + (size_t)((dtile[i] >> 4) * 172 + sch::p1_unit(18, dtile[i] & 15)) * 1024;
        if (lane < 16) *(v2u*)(fr + ((kb >> 1) * 16 + c) * 16 + (kb & 1) * 8) = v2u{pk(h1, h2), pk(v - h1 - h2, 0.f)};
        else if (lane < 32 && kb == 0) *(v4u*)(fr + (48 + c) * 16) = v4u{0u, 0u, 0u, 0u};
      }
    }
  }
  STAMP_END(ws, 1);
}

namespace sch {
#define LA_ 5
#define CAP_ 4
#define WST_ 2
#define STH0_ 96
#define STHD_ 9
#define PUBD_ 2
#define CHKD_ 3
constexpr int RINGP = 152, LA = LA_, PF = 3, CAP = CAP_, WST = WST_;
constexpr int N1 = 171, NU = N1 + 162, NS1 = 19, NS = 37, NR = 62;
constexpr int C_STH[9] = {0, 0, 1, 1, 1, 2, 2, 3, 3};
constexpr int s_sth(int G, int i) {
  int st = 11 - G;
  for (int k = 1; k <= i; ++k) { st += 1; int rdy = (k < 5 ? 11 : k < 7 ? 15 : 19) - G; if (st < rdy) st = rdy; }
  return st;
}
constexpr int s_pub(int c) { return s_sth(0, c == 0 ? 1 : c == 1 ? 4 : c == 2 ? 6 : 8) + PUBD_; }
constexpr int s_chk(int c) { return s_pub(c) + CHKD_; }
constexpr int S_X = NS1 + UF / 9;
constexpr int S_FL = S_X + 2;
constexpr int a2class(int i) { return i < 8 ? 0 : i < 20 ? 1 : i < 28 ? 2 : 3; }
constexpr int piece_lo(int U) { return U < N1 ? U : 172 + 2 * (U - N1); }
constexpr int piece_hi(int U) { return U < N1 ? U : 172 + 2 * (U - N1) + 1; }
constexpr int u_k(int U) { return U < 95 ? U / 5 : U < 133 ? (U - 95) / 2 : U < N1 ? (U - 133) / 2 : U < N1 + UF ? (U - N1) / 7 : (U - N1 - UF) / 2; }
constexpr int u_t(int U) { return U < 95 ? (U % 5 == 4 ? 8 : U % 5) : U < 133 ? 4 + ((U - 95) & 1) : U < N1 ? 6 + ((U - 133) & 1) : U < N1 + UF ? (U - N1) % 7 : 7 + ((U - N1 - UF) & 1); }
constexpr int kmax(int st) {
  int lo = st < NS1 ? 0 : N1, m = -1;
  for (int U = lo; U < 9 * st + 9; ++U) m = u_k(U) > m ? u_k(U) : m;
  return m > 17 ? 17 : m;
}
constexpr int need_round(int st) { int U = 9 * st + 8 + LA; return piece_hi(U > NU - 1 ? NU - 1 : U) >> 3; }
constexpr int gl_need(int r) { int st = 0; while (need_round(st) < r) ++st; return st; }
constexpr int a_need(int ph, int i) { int st = ph ? NS1 : 0; while (2 * kmax(st) + 1 < i) ++st; return st; }
enum { BAR = 0, GL, A1, A2, STH, POLL, XR, STP, HRES, FLL, VAR };
struct Op { int k, a, b, pos; };
struct Prog {
  Op pre[40]; int npre;
  Op ops[NS][24]; int nops[NS];
  int wait[NS];
  bool ok;
};
constexpr Prog build(int G) {
  Prog P{};
  P.ok = true;
  int cnt = 0, gl = -1, a1 = -1, a2 = -1;
  int glseq[NR] = {}, a1seq[36] = {}, a2seq[36] = {}, extra[NS + 8] = {};
  {
    int n = 0;
    auto pre = [&](int k, int a, int b, int wgt) { P.pre[n++] = Op{k, a, b, 0}; cnt += wgt; return cnt; };
    for (int q = 0; q < PF; ++q) {
      while (a1 < 2 * kmax(q) + 1) { ++a1; a1seq[a1] = pre(A1, a1 >> 1, a1 & 1, 1); if (q == 0 && a1 == 1) while (gl < need_round(0)) { ++gl; glseq[gl] = pre(GL, gl, 0, 1); } }
      while (gl < need_round(q)) { ++gl; glseq[gl] = pre(GL, gl, 0, 1); }
      if (q == 0) pre(BAR, 0, 0, 0);
    }
    P.npre = n;
  }
  for (int st = 0; st < NS; ++st) {
    if (gl < need_round(st) || (st < NS1 ? a1 : a2) < 2 * kmax(st) + 1) P.ok = false;
    int need = glseq[need_round(st)], s = st < NS1 ? a1seq[2 * kmax(st) + 1] : a2seq[2 * kmax(st) + 1];
    need = s > need ? s : need;
    need = extra[st] > need ? extra[st] : need;
    P.wait[st] = cnt - need > 63 ? 63 : cnt - need;
    Op ld[24] = {}; int nl = 0, fixed = 0;
    for (int i = 0; i < 9; ++i) if (s_sth(G, i) == st) fixed += WST;
    if (st == S_X) fixed += 4 * WST;
    {
      int g2 = gl, b1 = a1, b2 = a2;
      for (int budget = CAP - fixed;; --budget) {
        const int war = st == 0 ? (RINGP - 8) >> 3 : (piece_hi(9 * st - 1) + RINGP - 7) >> 3;
        const int BIG = 1000;
        int ng = (g2 < NR - 1 && g2 + 1 <= war) ? gl_need(g2 + 1) : BIG;
        int na = BIG;
        if (b1 < 35) na = a_need(0, b1 + 1);
        else if (b2 < 35 && st >= s_chk(a2class(b2 + 1))) na = a_need(1, b2 + 1);
        int m = ng < na ? ng : na;
        if (m == BIG || (budget <= 0 && m - st > 2 + G)) break;
        if (na <= ng) { if (b1 < 35) { ++b1; ld[nl++] = Op{A1, b1 >> 1, b1 & 1, 0}; } else { ++b2; ld[nl++] = Op{A2, b2 >> 1, b2 & 1, 0}; } }
        else { ++g2; ld[nl++] = Op{GL, g2, 0, 0}; }
      }
    }
    int n = 0;
    auto req = [&](int at, int seq) { if (extra[at] < seq) extra[at] = seq; };
    auto opp = [&](int pos, int k, int a, int b, int wgt) { P.ops[st][n++] = Op{k, a, b, pos}; cnt += wgt; return cnt; };
    auto op = [&](int k, int a, int b, int wgt) { return opp(G ? 9 : 0, k, a, b, wgt); };
    if (st == S_X) for (int i = 0; i < 7; ++i) opp(0, XR, (i + 1) % 7, 0, 0);
    for (int c = 0; c < 4; ++c) if (st == s_chk(c) - 1) req(s_chk(c), opp(0, POLL, c, 0, 1));
    if (st == NS - 2) req(NS - 1, opp(G ? 4 : 0, FLL, 0, 0, 1));
    if (st == S_X && G) for (int i = 0; i < 4; ++i) op(STP, i, 0, i == 3 ? 0 : 1);
    for (int il = 0; il < nl; ++il) {
      int q = op(ld[il].k, ld[il].a, ld[il].b, 1);
      if (ld[il].k == GL) glseq[++gl] = q; else if (ld[il].k == A1) a1seq[++a1] = q; else a2seq[++a2] = q;
    }
    if (st == S_X && !G) for (int i = 0; i < 4; ++i) op(STP, i, 0, i == 3 ? 0 : 1);
    for (int h = 0; h < 9; ++h) if (s_sth(G, h) == st) req(s_pub(C_STH[h]), op(STH, h, 0, 1));
    if (st == S_X - 2) { op(HRES, 0, 0, 1); op(HRES, 1, 0, 1); }
    if (st == 15) op(VAR, 0, 0, 0);
    P.nops[st] = n;
    if (n > 24) P.ok = false;
  }
  return P;
}
constexpr Prog PGS[2] = {build(0), build(1)};
static_assert(PGS[0].ok && PGS[1].ok, "schedule: an operand is needed before it is requested");
static_assert(UF % 9 == 0 && S_FL < NS - 1, "exchange must fit in the loop");
static_assert(piece_lo(9 * (S_X - 1)) % RINGP - (8 * NR) % RINGP >= 56, "exchange area overlaps live ring pieces");
constexpr int first_op(int G, int st, int i) { int n = 0; while (n < PGS[G].nops[st] && PGS[G].ops[st][n].pos < i) ++n; return n; }
}

static constexpr int L_X = ((8 * sch::NR) % sch::RINGP) * 1024;
static constexpr int L_RS = sch::RINGP * 1024;
static constexpr int L_FFN = L_RS + 512;

DEVI void spin_ge(v4u srd, unsigned soff, unsigned want) {
#pragma nounroll
  for (int spin = 0; spin < (1 << 18); ++spin) {
    unsigned v;
    asm volatile("buffer_load_dword %0, off, %1, %2 sc1\n\ts_waitcnt vmcnt(0)" : "=&v"(v) : "s"(srd), "s"(soff) : "memory");
    if ((unsigned)__builtin_amdgcn_readfirstlane(v) >= want) break;
  }
}

extern "C" __global__ __launch_bounds__(512) void ffn(char* __restrict__ ws, char* __restrict__ out) {
  using namespace sch;
  extern __shared__ __attribute__((aligned(16))) char sm[];
  asm volatile("" ::"s"(ws), "s"(out));
  const int tid = threadIdx.x, lane = tid & 63;
  const int w = __builtin_amdgcn_readfirstlane(tid >> 6);
  const unsigned lbase = (unsigned)(size_t)(LAS char*)sm;
  const int g = lane >> 4, c = lane & 15;
  const int bx = blockIdx.x & 7, bi = blockIdx.x >> 3;
  const int s = bx & 3, mt8 = (bx >> 2) * 4 + (bi >> 3), jl = bi & 7, j = 8 * s + jl;
  const int mt16 = 2 * mt8 + (jl >> 2), nt = jl & 3, wm = w & 3, wk = w >> 2;
  STAMP_DECL;
  const unsigned l16 = lane * 16;
  const v4u srd = {(unsigned)(size_t)ws, (unsigned)((size_t)ws >> 32) & 0xffffu, (unsigned)OFF_ST, 0x00020000u};
#define ABL 0
  const v4u srd0 = {srd.x, srd.y, 0u, srd.w};
  const v4u srdG = (ABL & 1) ? srd0 : srd, srdA = (ABL & 2) ? srd0 : srd, srdS = (ABL & 4) ? srd0 : srd;
  unsigned w1src = OFF_W1 + j * 172 * 1024 + w * 1024;
  unsigned w2src = OFF_W2 + (s * 4 + nt) * 324 * 1024 - 172 * 1024 + w * 1024;
  unsigned a1src = OFF_H + (16 * mt8 + 2 * w) * 18 * 1024;
  unsigned a2src0 = OFF_HID + ((8 * mt16 + 2 * wm + wk) * 144 + 36 * s + wk) * 1024;
  unsigned a2src1 = OFF_HID + ((8 * mt16 + 2 * wm + 1 - wk) * 144 + 36 * s + wk) * 1024;
  const unsigned hdst = OFF_HID + ((16 * mt8 + 2 * w) * 144 + 36 * s) * 1024;
  unsigned cnt = OFF_CTL + (mt8 * 4 + s) * 256;
  const unsigned inb = OFF_IN + (mt16 * 4 + nt) * 4 * INB;
  unsigned wf = OFF_WF + ((mt16 * 4 + nt) * 8 + w) * 128;
  unsigned ldsw = lbase + w * 1024;
  const int pr = jl >> 1, odd = jl & 1;
  const bool dovar = wk == (jl >> 2);
  const int mfin = 8 * mt16 + 2 * wm + wk;
  const int pcol = 144 * nt + 16 * (nt & 1) + 32 * s;
  const int scol = 144 * nt + ((nt & 1) ? 0 : 128);

  v4u A1r[19][2], A2r[18][2], Bq[LA + 1], hres, fl, q[3];
  v2u hres1, q1[3];
  unsigned pollv[4];
  f32x4 acc[9][2], acc2[9][2], sum[2], gram[2], xr[7];
  float mean[2];
#pragma unroll
  for (int i = 0; i < 9; ++i) { acc[i][0] = acc[i][1] = acc2[i][0] = acc2[i][1] = f32x4{0.f, 0.f, 0.f, 0.f}; }
  sum[0] = sum[1] = gram[0] = gram[1] = f32x4{0.f, 0.f, 0.f, 0.f};
  const v4u ones = {0x3f803f80u, 0x3f803f80u, 0x3f803f80u, 0x3f803f80u};
  unsigned bl[3] = {lbase + l16, lbase + l16 + 65536, lbase + l16 + 131072};
  pin(bl[0]); pin(bl[1]); pin(bl[2]);

  auto half = [&](f32x4 a) -> v2u { return v2u{relu2(pk(a[0], a[1])), relu2(pk(a[2], a[3]))}; };
  auto doop = [&](auto K, auto AA, auto BB) {
    constexpr int k = decltype(K)::v, a = decltype(AA)::v, b = decltype(BB)::v;
    if constexpr (k == BAR) asm volatile("s_barrier" ::: "memory");
    if constexpr (k == GL) {
      if constexpr (a == 21) w1src = w < 4 ? w1src : w2src;
      blds16<a * 8192, ((8 * a) % RINGP) * 1024>(srdG, l16, a <= 21 ? w1src : w2src, ldsw);
    }
    if constexpr (k == A1) bl16<(b * 18 + a) * 1024>(A1r[a][b], srdA, l16, a1src);
    if constexpr (k == A2) bl16c<a * 2048>(A2r[a][b], srdA, l16, b ? a2src1 : a2src0);
    if constexpr (k == HRES) {
      if constexpr (a == 0) bl16<(int)OFF_H>(hres, srd, l16, (unsigned)((mfin * 18 + (pcol >> 5)) * 1024));
      else bl8<(int)OFF_H>(hres1, srd, (((scol >> 3) & 3) + (g >> 1)) * 256 + c * 16 + (g & 1) * 8, (unsigned)((mfin * 18 + (scol >> 5)) * 1024));
    }
    if constexpr (k == STH) {
      if constexpr (a == 4) {
        v2u x = half(acc[8][0]), y = half(acc[8][1]);
        const bool sw = pr & 1;
        bs16c<16 * 1024>(v4u{sw ? y.x : x.x, sw ? y.y : x.y, sw ? x.x : y.x, sw ? x.y : y.y}, srdS, l16, hdst + (((pr ^ odd) & 1) * 144 + pr) * 1024);
      } else {
        constexpr int i = a < 4 ? a : a - 1, m = i & 1, p = i >> 1;
        v2u x = half(acc[2 * p][m]), y = half(acc[2 * p + 1][m]);
        bs16c<(m * 144 + kpair(p)) * 1024>(v4u{x.x, x.y, y.x, y.y}, srdS, l16, hdst + jl * 1024);
      }
    }
    if constexpr (k == VAR) {
      if (dovar) {
#pragma unroll
        for (int m = 0; m < 2; ++m) {
          const int r = c & 3;
          float sq = r == 0 ? gram[m][0] : r == 1 ? gram[m][1] : r == 2 ? gram[m][2] : gram[m][3];
          float var = rbf(fmaxf(sq * (1.0f / D) - mean[m] * mean[m], 0.f));
          if ((c >> 2) == g) *(float*)(sm + L_RS + (wm * 2 + m) * 64 + c * 4) = __builtin_amdgcn_rcpf(rbf(__builtin_amdgcn_sqrtf(rbf(var + 1e-5f))));
        }
      }
    }
    if constexpr (k == POLL) bl4c<a * 8192>(pollv[a], srd, 0u, cnt);
    if constexpr (k == FLL) bl16c<0>(fl, srd, 0u, wf);
    if constexpr (k == XR) xr[a] = *(const f32x4*)(sm + L_X + (w ^ 4) * 7168 + a * 1024 + l16);
    if constexpr (k == STP) {
      if constexpr (a < 3) {
        f32x4 x = acc2[1 + 2 * a][0] + xr[1 + 2 * a], y = acc2[2 + 2 * a][0] + xr[2 + 2 * a];
        bs16c<(2 - a) * 8192>(v4u{pk(x[0], x[1]), pk(x[2], x[3]), pk(y[0], y[1]), pk(y[2], y[3])}, srdS, l16, inb + ((s + 1 + a) & 3) * INB + (2 * wm + wk) * 1024);
      } else {
        acc2[0][0] += xr[0];
        if (wm != s) bs8c<24576>(v2u{pk(acc2[0][0][0], acc2[0][0][1]), pk(acc2[0][0][2], acc2[0][0][3])}, srd, lane * 8,
                                 inb + wm * INB + ((s - wm - 1) & 3) * 1024 + wk * 512);
      }
    }
  };
  auto rdB = [&](auto UU) {
    constexpr int U = decltype(UU)::v;
    constexpr int off = (piece_lo(U) % RINGP) * 1024;
    if constexpr (U == N1) {
#pragma unroll
      for (int i = 0; i < 3; ++i) { bl[i] += wk * 1024; pin(bl[i]); }
    }
    if constexpr (U < NU) Bq[U % (LA + 1)] = *(const LAS v4u*)(size_t)(bl[off >> 16] + (off & 65535));
  };

  auto run = [&](auto GG) {
  constexpr int G = decltype(GG)::v;
  if constexpr (G == 1) __builtin_amdgcn_s_setprio(PRIO);
  sfor<0, PGS[G].npre>([&](auto I) { constexpr Op o = PGS[G].pre[decltype(I)::v]; doop(IC<o.k>{}, IC<o.a>{}, IC<o.b>{}); });
  sfor<0, NS>([&](auto SS) {
    constexpr int st = decltype(SS)::v;
    auto ops_at = [&](auto PP) {
      constexpr int p = decltype(PP)::v;
      sfor<first_op(G, st, p), first_op(G, st, p + 1)>([&](auto I) { constexpr Op o = PGS[G].ops[st][decltype(I)::v]; doop(IC<o.k>{}, IC<o.a>{}, IC<o.b>{}); });
    };
    if constexpr (st == S_FL) {
      asm volatile("s_waitcnt vmcnt(0)" ::: "memory");
      if (lane == 0) __hip_atomic_store((GAS unsigned*)(ws + wf + s * 4), 1u, RLX_AGENT);
    }
    if constexpr (st == S_X) asm volatile("s_waitcnt vmcnt(%0) lgkmcnt(0)\n\ts_barrier" ::"i"(PGS[G].wait[st]) : "memory");
    else asm volatile("s_waitcnt vmcnt(%0)\n\ts_barrier" ::"i"(PGS[G].wait[st]) : "memory");
    sfor<(st == 0 || st == NS1) ? 0 : kmax(st - 1) + 1, kmax(st) + 1>([&](auto KK) {
      constexpr int k = decltype(KK)::v;
      if constexpr (st < NS1) { pin(A1r[k][0]); pin(A1r[k][1]); }
      else {
        pin(A2r[k][0]); pin(A2r[k][1]);
        if constexpr (k == 8 || k == 9) {
          v4u x = A2r[k][0], y = A2r[k][1];
          A2r[k][0] = v4u{x.x, x.y, y.x, y.y}; A2r[k][1] = v4u{x.z, x.w, y.z, y.w};
        }
      }
    });
    sfor<0, 4>([&](auto CC) {
      constexpr int cl = decltype(CC)::v;
      if constexpr (st == s_pub(cl) && G == 0) { if ((tid & 255) == 0) __hip_atomic_fetch_add((GAS unsigned*)(ws + cnt + cl * 8192), 1u, RLX_AGENT); }
      if constexpr (st == s_chk(cl)) {
        pin(pollv[cl]);
        if ((unsigned)__builtin_amdgcn_readfirstlane(pollv[cl]) < 8u) spin_ge(srd, cnt + cl * 8192, 8u);
      }
    });
    if constexpr (st == 0) sfor<0, LA>(rdB);
    if constexpr (st == NS - 1) {
      pin(fl);
#pragma nounroll
      for (int spin = 0; spin < (1 << 18) && __builtin_amdgcn_readfirstlane(fl.x + fl.y + fl.z + fl.w) != 4; ++spin)
        asm volatile("buffer_load_dwordx4 %0, off, %1, %2 sc1\n\ts_waitcnt vmcnt(0)" : "=&v"(fl) : "s"(srd), "s"(wf) : "memory");
      ST(10);
      const unsigned myin = inb + s * INB;
      sfor<0, 3>([&](auto I) { bl16c<decltype(I)::v * 8192>(q[decltype(I)::v], srd, l16, myin + (2 * wm + wk) * 1024); });
      sfor<0, 3>([&](auto I) { bl8c<24576 + decltype(I)::v * 1024>(q1[decltype(I)::v], srd, wm == s ? lane * 8 : 0, myin + wk * 512); });
    }
    ops_at(IC<0>{});
    if constexpr (st >= 11 && st <= 14) {
      if (dovar) {
        sfor<(st - 11) * 5, (st == 14 ? 18 : (st - 10) * 5)>([&](auto KK) {
          constexpr int k = decltype(KK)::v;
          gram[0] = mfma(A1r[k][0], A1r[k][0], gram[0]);
          gram[1] = mfma(A1r[k][1], A1r[k][1], gram[1]);
        });
        pin(gram[0]); pin(gram[1]);
      }
    }
    __builtin_amdgcn_sched_barrier(0);
    sfor<0, 9>([&](auto TT) {
      constexpr int i = decltype(TT)::v, U = 9 * st + i, k = u_k(U), t = u_t(U);
      if constexpr (U == 88) {
#pragma unroll
        for (int m = 0; m < 2; ++m) { mean[m] = sum[m][0] * (1.0f / D); unsigned nm = pk(-mean[m], -mean[m]); A1r[18][m] = v4u{nm, nm, nm, nm}; }
      }
      if constexpr (st < NS1) {
        acc[t][0] = mfma(Bq[U % (LA + 1)], A1r[k][0], acc[t][0]);
        acc[t][1] = mfma(Bq[U % (LA + 1)], A1r[k][1], acc[t][1]);
        pin(acc[t][0]); pin(acc[t][1]);
        if constexpr (U < 90 && t == 0) { sum[0] = mfma(ones, A1r[k][0], sum[0]); sum[1] = mfma(ones, A1r[k][1], sum[1]); pin(sum[0]); pin(sum[1]); }
      } else {
        acc2[t][0] = mfma(Bq[U % (LA + 1)], A2r[k][0], acc2[t][0]);
        acc2[t][1] = mfma(Bq[U % (LA + 1)], A2r[k][1], acc2[t][1]);
        pin(acc2[t][0]); pin(acc2[t][1]);
      }
      rdB(IC<U + LA>{});
      if constexpr (U > N1 + UF - 7 && U < N1 + UF) *(f32x4*)(sm + L_X + w * 7168 + (t - 1) * 1024 + l16) = acc2[t - 1][1];
      if constexpr (U == N1 + UF - 1) *(f32x4*)(sm + L_X + w * 7168 + t * 1024 + l16) = acc2[t][1];
      ops_at(IC<i + 1>{});
      __builtin_amdgcn_sched_barrier(0);
    });
  });
  ST(11);
  *(f32x4*)(sm + L_X + w * 7168 + l16) = acc2[7][1];
  *(f32x4*)(sm + L_X + w * 7168 + 1024 + l16) = acc2[8][1];
  asm volatile("s_waitcnt lgkmcnt(0)\n\ts_barrier" ::: "memory");
  const f32x4 o0 = acc2[7][0] + *(const f32x4*)(sm + L_X + (w ^ 4) * 7168 + l16);
  const f32x4 o1 = acc2[8][0] + *(const f32x4*)(sm + L_X + (w ^ 4) * 7168 + 1024 + l16);
  const float rs = *(const float*)(sm + L_RS + (2 * wm + wk) * 64 + c * 4);
  ST(12);
  asm volatile("s_waitcnt vmcnt(0)" : "+v"(q[0]), "+v"(q[1]), "+v"(q[2]), "+v"(q1[0]), "+v"(q1[1]), "+v"(q1[2]), "+v"(hres), "+v"(hres1)::"memory");
  ST(13);
  const f32x2 rr = {rs, rs};
  {
    f32x2 a = (f32x2{o0[0], o0[1]} + up2(q[1].x)) + (up2(q[0].x) + up2(q[2].x));
    f32x2 b = (f32x2{o0[2], o0[3]} + up2(q[1].y)) + (up2(q[0].y) + up2(q[2].y));
    f32x2 cc = (f32x2{o1[0], o1[1]} + up2(q[1].z)) + (up2(q[0].z) + up2(q[2].z));
    f32x2 d = (f32x2{o1[2], o1[3]} + up2(q[1].w)) + (up2(q[0].w) + up2(q[2].w));
    v4u r = {pk2(up2(hres.x) + up2(pk2(a * rr))), pk2(up2(hres.y) + up2(pk2(b * rr))), pk2(up2(hres.z) + up2(pk2(cc * rr))), pk2(up2(hres.w) + up2(pk2(d * rr)))};
    *(v4u*)(out + (size_t)(mfin * 16 + c) * ROWB + (pcol + 8 * g) * 2) = r;
  }
  if (wm == s) {
    f32x2 a = (f32x2{acc2[0][0][0], acc2[0][0][1]} + up2(q1[1].x)) + (up2(q1[0].x) + up2(q1[2].x));
    f32x2 b = (f32x2{acc2[0][0][2], acc2[0][0][3]} + up2(q1[1].y)) + (up2(q1[0].y) + up2(q1[2].y));
    v2u r = {pk2(up2(hres1.x) + up2(pk2(a * rr))), pk2(up2(hres1.y) + up2(pk2(b * rr)))};
    *(v2u*)(out + (size_t)(mfin * 16 + c) * ROWB + (scol + 4 * g) * 2) = r;
  }
  STAMP_END(ws, 2);
  };
  if (wk) run(IC<0>{}); else run(IC<1>{});
}

static constexpr int K0_K = 0, K0_V = 11264, K0_X = K0_V + 10240, K0_SK = K0_X + 10240, K0_O = K0_SK + 3072, K0_PF = K0_O + 72 * 144, K0_LDS = K0_PF + 9216;

extern "C" __global__ __launch_bounds__(704) void k0_prep(const char* __restrict__ xg, const char* __restrict__ kg, const char* __restrict__ vg,
                                               const char* __restrict__ skg, const char* __restrict__ kfg, char* __restrict__ ws) {
  __shared__ __attribute__((aligned(16))) char sm[K0_LDS];
  asm volatile("" ::"s"(xg), "s"(kg), "s"(vg), "s"(skg), "s"(kfg), "s"(ws));
  const int a = blockIdx.x, tid = threadIdx.x, lane = tid & 63;
  const int w = __builtin_amdgcn_readfirstlane(tid >> 6);
  const unsigned lbase = (unsigned)(size_t)(LAS char*)sm;
  const int tb = a >> 1, hf = a & 1;
  STAMP_DECL;
  int row = tid / 37, col = tid - row * 37;
  col = col > 35 ? 35 : col;
  const int gr = 16 * tb - 3 + (row > 18 ? 18 : row);
  const unsigned vo = (16 * tb + (row > 15 ? 15 : row)) * ROWB + hf * 576 + col * 16;
  glds16(kg, (gr < 0 ? 0 : gr) * ROWB + hf * 576 + col * 16, lbase + K0_K + w * 1024);
  if (w < 10) glds16(vg, vo, lbase + K0_V + w * 1024);
  if (w < 3) glds16(skg, hf * 2304 + (tid < 144 ? tid : 143) * 16, lbase + K0_SK + w * 1024);
  if (w < 10) glds16(xg, vo, lbase + K0_X + w * 1024);
  if (w < 9) {
    glds16(kfg, (18 * (a & 7)) * 16384 + (a >> 3) * 9216 + tid * 16, lbase + K0_PF + w * 1024);
    asm volatile("s_waitcnt vmcnt(1)\n\ts_barrier" ::: "memory");
    ST(2);
    const int rp = tid & 7, cgl = tid >> 3;
    {
      v2u x0 = *(const v2u*)(sm + K0_X + (2 * rp) * 592 + cgl * 8), x1 = *(const v2u*)(sm + K0_X + (2 * rp + 1) * 592 + cgl * 8);
      *(v4u*)(ws + OFF_XT + (size_t)(72 * hf + cgl) * 16384 + tb * 128 + rp * 16) = v4u{x0.x, x0.y, x1.x, x1.y};
    }
    v2u kk[5], vv[2];
#pragma unroll
    for (int i = 0; i < 5; ++i) {
      v2u x = *(const v2u*)(sm + K0_K + (2 * rp + i) * 592 + cgl * 8);
      bool z = (16 * tb + 2 * rp + i) < 3;
      kk[i].x = z ? 0u : x.x; kk[i].y = z ? 0u : x.y;
    }
    vv[0] = *(const v2u*)(sm + K0_V + (2 * rp) * 592 + cgl * 8);
    vv[1] = *(const v2u*)(sm + K0_V + (2 * rp + 1) * 592 + cgl * 8);
    v4u s0 = *(const v4u*)(sm + K0_SK + cgl * 32), s1 = *(const v4u*)(sm + K0_SK + cgl * 32 + 16);
    unsigned sw[8] = {s0.x, s0.y, s0.z, s0.w, s1.x, s1.y, s1.z, s1.w};
    const int rot = (tb >> 1) & 3;
#pragma unroll
    for (int ch = 0; ch < 4; ++ch) {
      float kf[5], sk[4];
#pragma unroll
      for (int i = 0; i < 5; ++i) { unsigned u = (ch & 2) ? kk[i].y : kk[i].x; kf[i] = (ch & 1) ? hi2f(u) : lo2f(u); }
      sk[0] = lo2f(sw[2 * ch]); sk[1] = hi2f(sw[2 * ch]); sk[2] = lo2f(sw[2 * ch + 1]); sk[3] = hi2f(sw[2 * ch + 1]);
      unsigned acc = pk(kf[0] * sk[3], kf[1] * sk[3]);
#pragma unroll
      for (int j = 1; j < 4; ++j) acc = pk2(up2(acc) + up2(pk(kf[j] * sk[3 - j], kf[j + 1] * sk[3 - j])));
      unsigned u0 = (ch & 2) ? vv[0].y : vv[0].x, u1 = (ch & 2) ? vv[1].y : vv[1].x;
      float v0 = (ch & 1) ? hi2f(u0) : lo2f(u0), v1 = (ch & 1) ? hi2f(u1) : lo2f(u1);
      *(unsigned*)(sm + K0_O + cgl * 144 + (rp >> 2) * 64 + ((ch + rot) & 3) * 16 + (3 - (rp & 3)) * 4) = pk(hi2f(acc) * v1, lo2f(acc) * v0);
    }
    ST(3);
    asm volatile("" ::: "memory");
    v4u o = *(const v4u*)(sm + K0_O + cgl * 144 + rp * 16);
    *(v4u*)(ws + OFF_KV + (size_t)(72 * hf + cgl) * 16384 + tb * 128 + rp * 16) = o;
  } else {
    const int r = 8 * a + 4 * (w - 9) + (lane >> 4), p = lane & 15;
    v2u xr[9];
#pragma unroll
    for (int i = 0; i < 9; ++i) gld8(xr[i], xg, r * ROWB + (p + 16 * i) * 8);
    {
      unsigned lo, hi, len;
      asm volatile("s_getpc_b64 vcc\n\ts_add_u32 %0, vcc_lo, k1_conv@rel32@lo+4\n\ts_addc_u32 %1, vcc_hi, k1_conv@rel32@hi+12\n\ts_mov_b32 %2, k0_prep-k1_conv"
                   : "=s"(lo), "=s"(hi), "=s"(len)::"scc", "vcc");
      const v4u cs = {lo, hi & 0xffffu, len > 0x40000u ? 0u : len, 0x00020000u};
      const v4u wsd = {(unsigned)(size_t)ws, (unsigned)((size_t)ws >> 32) & 0xffffu, (unsigned)OFF_ST, 0x00020000u};
      asm volatile("s_mov_b32 m0, %2\n\ts_nop 0\n\tbuffer_load_dword %0, %1, 0 offen lds" ::"v"((((a >> 3) * 2 + w - 9) * 64 + lane) * 128), "s"(cs), "s"(lbase + K0_PF) : "memory");
      asm volatile("buffer_load_dword %0, %1, 0 offen lds" ::"v"(lane * 0x100000 + ((a >> 3) * 2 + w - 9) * 0x4000), "s"(wsd) : "memory");
      asm volatile("s_waitcnt vmcnt(11)\n\ts_barrier\n\ts_waitcnt vmcnt(2)" ::: "memory");
    }
    ST(2);
#pragma unroll
    for (int i = 0; i < 9; ++i) pin(xr[i]);
    if (w == 9) {
      if (lane == 8 && a < 128) *(unsigned*)(ws + OFF_CTL + a * 256) = 0u;
      if (lane >= 16 && lane < 18) *(v4u*)(ws + OFF_WF + (a * 2 + lane - 16) * 128) = v4u{0u, 0u, 0u, 0u};
    }
    float s = 0.f, s2 = 0.f;
#pragma unroll
    for (int i = 0; i < 9; ++i) { s = sum2(xr[i].x, s); s2 = sum2(xr[i].y, s2); }
    const float mean = row16_sum(s + s2) * (1.0f / D);
    f32x2 q = {0.f, 0.f}, q2 = {0.f, 0.f};
    const f32x2 mm = {mean, mean};
#pragma unroll
    for (int i = 0; i < 9; ++i) { f32x2 d0 = up2(xr[i].x) - mm, d1 = up2(xr[i].y) - mm; q += d0 * d0; q2 += d1 * d1; }
    q += q2;
    const float var = rbf(row16_sum(q.x + q.y) * (1.0f / D));
    if (p == 0) *(unsigned*)(ws + OFF_ST1 + (((r >> 8) * 64 + (r & 63)) * 4 + ((r >> 6) & 3)) * 4) = pk(mean, sqrtf(rbf(var + 1e-5f)));
  }
  STAMP_END(ws, 0);
}

extern "C" void kernel_launch(void* const* d_in, const int* in_sizes, int n_in, void* d_out, int out_size, void* d_ws, size_t ws_size,
                              hipStream_t stream) {
  char* ws = (char*)d_ws;
  (void)hipFuncSetAttribute((const void*)k1_conv, hipFuncAttributeMaxDynamicSharedMemorySize, L_K1);
  (void)hipFuncSetAttribute((const void*)ffn, hipFuncAttributeMaxDynamicSharedMemorySize, L_FFN);
  k0_prep<<<256, 704, 0, stream>>>((const char*)d_in[0], (const char*)d_in[1], (const char*)d_in[2], (const char*)d_in[3], (const char*)d_in[4], ws);
  k1_conv<<<252, 512, L_K1, stream>>>((const char*)d_in[4], (const u16*)d_in[5], (const char*)d_in[7], (const char*)d_in[8], ws);
  ffn<<<256, 512, L_FFN, stream>>>(ws, (char*)d_out);
}
```

```cpp
#include <hip/hip_runtime.h>
#include <hip/hip_bf16.h>
#include <stdint.h>

typedef unsigned v4u __attribute__((ext_vector_type(4)));
typedef unsigned v2u __attribute__((ext_vector_type(2)));
typedef float f32x4 __attribute__((ext_vector_type(4)));
typedef float f32x2 __attribute__((ext_vector_type(2)));
typedef __bf16 bf16x2 __attribute__((ext_vector_type(2)));
typedef __bf16 bf16x8 __attribute__((ext_vector_type(8)));
typedef short s16x4 __attribute__((ext_vector_type(4)));
typedef short s16x2 __attribute__((ext_vector_type(2)));
#define LAS __attribute__((address_space(3)))
#define DEVI __device__ __forceinline__

static constexpr int S = 2048, D = 576, F = 4608;
static constexpr int ROWB = D * 2;

static constexpr int OFF_EP = 0;
static constexpr int OFF_F0 = 1024;
static constexpr int OFF_FW = OFF_F0 + 144 * 128;
static constexpr int OFF_FV = OFF_FW + 112 * 128;
static constexpr int OFF_FH = OFF_FV + 112 * 128;
static constexpr int OFF_FC = OFF_FH + 1152 * 128;
static constexpr int OFF_FP = OFF_FC + 1024 * 128;
static constexpr int OFF_ST1 = OFF_FP + 768 * 128;
static constexpr int OFF_KV = OFF_ST1 + 16384;
static constexpr int OFF_XT = OFF_KV + S * D * 2;
static constexpr int OFF_H = OFF_XT + S * D * 2;
static constexpr int OFF_W1 = OFF_H + S * D * 2;
static constexpr int OFF_W2 = OFF_W1 + 32 * 172 * 1024;
static constexpr int OFF_HID = OFF_W2 + D * F * 2;
static constexpr int OFF_IN = OFF_HID + S * F * 2;
static constexpr int INB = 63 * 1024;
static constexpr int OFF_ST = OFF_IN + 256 * INB;

DEVI float lo2f(unsigned u) { return __uint_as_float(u << 16); }
DEVI float hi2f(unsigned u) { return __uint_as_float(u & 0xffff0000u); }
DEVI f32x2 up2(unsigned u) { return f32x2{lo2f(u), hi2f(u)}; }
DEVI unsigned pk2(f32x2 v) { return __builtin_bit_cast(unsigned, __builtin_convertvector(v, bf16x2)); }
DEVI unsigned pk(float a, float b) { return pk2(f32x2{a, b}); }
DEVI f32x2 fma2(f32x2 a, f32x2 b, f32x2 c) { return __builtin_elementwise_fma(a, b, c); }
DEVI float sum2(unsigned u, float c) { const bf16x2 one = {(__bf16)1.0f, (__bf16)1.0f}; return __builtin_amdgcn_fdot2_f32_bf16(__builtin_bit_cast(bf16x2, u), one, c, false); }
DEVI float rbf(float a) { return lo2f(pk(a, a)); }
DEVI unsigned relu2(unsigned u) { s16x2 z = {0, 0}; return __builtin_bit_cast(unsigned, __builtin_elementwise_max(__builtin_bit_cast(s16x2, u), z)); }
DEVI f32x4 mfma(v4u a, v4u b, f32x4 c) {
  return __builtin_amdgcn_mfma_f32_16x16x32_bf16(__builtin_bit_cast(bf16x8, a), __builtin_bit_cast(bf16x8, b), c, 0, 0, 0);
}
template <int N> struct IC { static constexpr int v = N; };
template <int I, int N, class Fn> DEVI void sfor(Fn&& f) { if constexpr (I < N) { f(IC<I>{}); sfor<I + 1, N>(f); } }
template <int CTRL> DEVI float dpp_add(float v) {
  return v + __builtin_bit_cast(float, __builtin_amdgcn_update_dpp(0, __builtin_bit_cast(int, v), CTRL, 0xf, 0xf, true));
}
DEVI float row16_sum(float v) { v = dpp_add<0xB1>(v); v = dpp_add<0x4E>(v); v = dpp_add<0x141>(v); return dpp_add<0x140>(v); }
DEVI v4u mksrd(const void* p, unsigned bytes) { return v4u{(unsigned)(size_t)p, (unsigned)((size_t)p >> 32) & 0xffffu, bytes, 0x00020000u}; }

#define BUFLD(name, T, text)                                                                                     \
  template <int IMM, class U> DEVI void name(T& d, v4u srd, unsigned voff, U&& soff) {                           \
    soff = __builtin_amdgcn_readfirstlane(soff);                                                                 \
    asm volatile("" : "+s"(soff));                                                                               \
    asm volatile(text : "=v"(d) : "v"(voff), "s"(srd), "s"(soff + IMM) : "memory");                              \
  }
#define BUFST(name, T, text)                                                                                     \
  template <int IMM, class U> DEVI void name(T d, v4u srd, unsigned voff, U&& soff) {                            \
    soff = __builtin_amdgcn_readfirstlane(soff);                                                                 \
    asm volatile("" : "+s"(soff));                                                                               \
    asm volatile(text ::"v"(d), "v"(voff), "s"(srd), "s"(soff + IMM) : "memory");                                \
  }
BUFLD(bl16, v4u, "buffer_load_dwordx4 %0, %1, %2, %3 offen")
BUFLD(bl8, v2u, "buffer_load_dwordx2 %0, %1, %2, %3 offen")
BUFLD(bl16c, v4u, "buffer_load_dwordx4 %0, %1, %2, %3 offen sc1")
BUFLD(bl4c, unsigned, "buffer_load_dword %0, %1, %2, %3 offen sc1")
BUFST(bs16c, v4u, "buffer_store_dwordx4 %0, %1, %2, %3 offen sc1\n\ts_nop 1")
BUFST(bs8c, v2u, "buffer_store_dwordx2 %0, %1, %2, %3 offen sc1")
BUFST(bs4c, unsigned, "buffer_store_dword %0, %1, %2, %3 offen sc1")
template <int IMM, int LIMM, class U, class V> DEVI void blds16(v4u srd, unsigned voff, U&& soff, V&& lds) {
  asm volatile("" : "+s"(soff), "+s"(lds));
  asm volatile("s_mov_b32 m0, %3\n\ts_nop 0\n\tbuffer_load_dwordx4 %0, %1, %2 offen lds" ::"v"(voff), "s"(srd), "s"(soff + IMM), "s"(lds + LIMM) : "memory");
}
template <int IMM, int LIMM, class U, class V> DEVI void blds16c(v4u srd, unsigned voff, U&& soff, V&& lds) {
  asm volatile("" : "+s"(soff), "+s"(lds));
  asm volatile("s_mov_b32 m0, %3\n\ts_nop 0\n\tbuffer_load_dwordx4 %0, %1, %2 offen sc1 lds" ::"v"(voff), "s"(srd), "s"(soff + IMM), "s"(lds + LIMM) : "memory");
}
DEVI void blds4c(v4u srd, unsigned voff, unsigned soff, unsigned lds) {
  asm volatile("s_mov_b32 m0, %3\n\ts_nop 0\n\tbuffer_load_dword %0, %1, %2 offen sc1 lds" ::"v"(voff), "s"(srd), "s"(soff), "s"(lds) : "memory");
}
DEVI void blds4(v4u srd, unsigned voff, unsigned lds) {
  asm volatile("s_mov_b32 m0, %2\n\ts_nop 0\n\tbuffer_load_dword %0, %1, 0 offen lds" ::"v"(voff), "s"(srd), "s"(lds) : "memory");
}
template <class T> DEVI void pin(T& x) { asm volatile("" : "+v"(x)); }
DEVI void lds_rd64(v2u& d, unsigned addr, int off) { asm volatile("ds_read_b64 %0, %1 offset:%2" : "=v"(d) : "v"(addr), "i"(off) : "memory"); }
DEVI void lds_rd128(v4u& d, unsigned addr, int off) { asm volatile("ds_read_b128 %0, %1 offset:%2" : "=v"(d) : "v"(addr), "i"(off) : "memory"); }
DEVI void set_flag(v4u srd, unsigned off, unsigned tag, int lane) { bs4c<0>(tag, srd, lane < 32 ? lane * 4 : 0x80000000u, off); }
template <int N> DEVI void waitv(v4u& x) { asm volatile("s_waitcnt vmcnt(%1)" : "+v"(x) : "i"(N) : "memory"); }
DEVI void reload3(v4u& a, v4u& b, v4u& c, v4u srd, unsigned voff, unsigned soff) {
  asm volatile("buffer_load_dwordx4 %0, %3, %4, %5 offen sc1\n\tbuffer_load_dwordx4 %1, %3, %4, %6 offen sc1\n\tbuffer_load_dwordx4 %2, %3, %4, %7 offen sc1\n\ts_waitcnt vmcnt(0)"
               : "=&v"(a), "=&v"(b), "=&v"(c) : "v"(voff), "s"(srd), "s"(soff + 4 * 8192), "s"(soff + 5 * 8192), "s"(soff + 6 * 8192) : "memory");
}
DEVI void reload5(v4u& a, v4u& b, v4u& c, v4u& d, v4u& e, v4u srd, unsigned voff, unsigned soff) {
  asm volatile("buffer_load_dwordx4 %0, %5, %6, %7 offen sc1\n\tbuffer_load_dwordx4 %1, %5, %6, %8 offen sc1\n\tbuffer_load_dwordx4 %2, %5, %6, %9 offen sc1\n\t"
               "buffer_load_dwordx4 %3, %5, %6, %10 offen sc1\n\tbuffer_load_dwordx4 %4, %5, %6, %11 offen sc1\n\ts_waitcnt vmcnt(0)"
               : "=&v"(a), "=&v"(b), "=&v"(c), "=&v"(d), "=&v"(e)
               : "v"(voff), "s"(srd), "s"(soff + 2 * 8192), "s"(soff + 3 * 8192), "s"(soff + 4 * 8192), "s"(soff + 5 * 8192), "s"(soff + 6 * 8192) : "memory");
}
DEVI void wait_flags_v(v4u srd, unsigned off, unsigned vo, unsigned tag) {
#pragma nounroll
  for (int spin = 0; spin < (1 << 17); ++spin) {
    unsigned v;
    asm volatile("buffer_load_dword %0, %1, %2, %3 offen sc1\n\ts_waitcnt vmcnt(0)" : "=&v"(v) : "v"(vo), "s"(srd), "s"(off) : "memory");
    vo = v == tag ? 0x80000000u : vo;
    if (__all((int)vo < 0)) break;
    __builtin_amdgcn_s_sleep(2);
  }
}
template <int N> DEVI void wait_flags(v4u srd, unsigned off, unsigned tag, int lane) { wait_flags_v(srd, off, lane < N ? lane * 128 : 0x80000000u, tag); }
DEVI void wait_stats(v4u& v, v4u srd, unsigned vo, unsigned off, unsigned tag) {
#pragma nounroll
  for (int spin = 0; spin < (1 << 17) && !__all(v.y == tag && v.w == tag); ++spin)
    asm volatile("buffer_load_dwordx4 %0, %1, %2, %3 offen sc1\n\ts_waitcnt vmcnt(0)" : "=&v"(v) : "v"(vo), "s"(srd), "s"(off) : "memory");
}

namespace sch {
#define HD __host__ __device__ constexpr
constexpr int NA = 1, WA = 2 * NA + 1;
HD int p1_unit(int kl, int tau) { return tau < 2 * NA ? WA * kl + tau : tau == 8 ? WA * kl + 2 * NA : 19 + 38 * (tau >> 1) + 2 * kl + (tau & 1); }
HD int p2_unit(int kk, int tp) {
  return tp >= 5 ? 18 * tp + kk : kk < 12 ? 5 * kk + tp : tp == 4 ? 72 + kk : kk < 15 ? 12 + 4 * kk + tp : 57 + 3 * tp + kk;
}
HD int p2_kk(int d, int p) { return d == 0 ? p : p == 0 ? 3 + d : 5 + 3 * p + d; }

constexpr int RINGP = 152, LA = 4, CAP = 4, WST = 3, TA = 4;
constexpr int N1 = 171, NU = N1 + 162, NS1 = 19, NS = 37, NR = 42;
struct UMap { int k[NU], t[NU], km[NU]; };
constexpr UMap mkmap() {
  UMap M{};
  for (int k = 0; k < 19; ++k) for (int t = 0; t < 9; ++t) { M.k[p1_unit(k, t)] = k; M.t[p1_unit(k, t)] = t; }
  for (int k = 0; k < 18; ++k) for (int t = 0; t < 9; ++t) { M.k[N1 + p2_unit(k, t)] = k; M.t[N1 + p2_unit(k, t)] = t; }
  for (int U = 0; U < NU; ++U) { int m = (U == 0 || U == N1) ? -1 : M.km[U - 1]; m = M.k[U] > m ? M.k[U] : m; M.km[U] = m > 17 ? 17 : m; }
  return M;
}
constexpr UMap UM = mkmap();
constexpr int u_k(int U) { return UM.k[U]; }
constexpr int u_t(int U) { return UM.t[U]; }
constexpr int c_sth(int i) { return i < 3 ? 0 : (i - 1) >> 1; }
constexpr int s_sth(int G, int i) {
  int st = 0;
  for (int k = 0; k <= i; ++k) { st += k == 8 ? 0 : 1; int rdy = (57 + 38 * (c_sth(k) < NA ? NA - 1 : c_sth(k)) + 8) / 9 - G; if (st < rdy) st = rdy; }
  return st;
}
constexpr int s_pub(int c) { return s_sth(0, 2 + 2 * c) + (c == 3 ? 3 : 3); }
constexpr int s_chk(int c) { return s_pub(c) + (c == 3 ? 3 : 4); }
constexpr int s_stp(int G, int tp) {
  int st = (N1 + p2_unit(17, tp) + 9) / 9 - G;
  if (tp >= 2 && st <= s_stp(G, tp - 2)) st = s_stp(G, tp - 2) + 1;
  return st;
}
constexpr int TB = 1;
constexpr int S_FB = s_stp(0, TB) + 2, S_QB = S_FB + 4;
constexpr int S_FA = s_stp(0, TA) + 2, S_QA = S_FA + 4;
constexpr int NLATE = 7 - TA;
constexpr int s_ql(int i) { return s_stp(0, i) + 3; }
constexpr int a2_kk(int i) { return i < 6 ? 4 + i / 2 : i < 9 ? (i == 6 ? 7 : 8) : 9 + (i - 9) / 2; }
constexpr int a2_b(int i) { return i < 6 ? i & 1 : i < 9 ? (i == 7 ? 0 : 1) : (i - 9) & 1; }
constexpr int a2_last(int kk) { int m = -1; for (int i = 0; i < 27; ++i) if (a2_kk(i) <= kk) m = i; return m; }
constexpr bool a2_loaded(int kk) { return kk >= 4; }
constexpr int a2class(int i) { return i < 9 ? 0 : 1 + (i - 9) / 6; }
constexpr int piece(int U) { return U < N1 ? U : U + 1; }
constexpr int kmaxu(int U1) { return UM.km[U1]; }
constexpr int alast(int st) { return st < NS1 ? 2 * kmaxu(9 * st + 8) + 1 : a2_last(kmaxu(9 * st + 8)); }
constexpr bool kfirst(int U) { return u_k(U) < 18 && (U == 0 || U == N1 || kmaxu(U) > kmaxu(U - 1)); }
constexpr int need_round(int st) { int U = 9 * st + 8 + LA; return piece(U > NU - 1 ? NU - 1 : U) >> 3; }
constexpr int gl_need(int r) { int st = 0; while (need_round(st) < r) ++st; return st; }
constexpr int a_need(int ph, int i) { int st = ph ? NS1 : 0; while (alast(st) < i) ++st; return st; }
enum { GL, A1, A2, STH, POLL, QL, STP, HRES, FLL, VAR, WAITH, POLLV, MARK, WAITW };
constexpr int S_MK[3] = {13, 14, 17};
constexpr int S_FM = 17 + 4, S_CM = s_stp(1, TA + 1);
constexpr int S_V = 9;
struct Op { int k, a, b, pos; };
struct Prog {
  Op pre[40]; int npre;
  Op ops[NS][24]; int nops[NS];
  int wait[NS], awt[NU], qw[9];
  bool ok;
};
constexpr Prog build(int G) {
  Prog P{};
  P.ok = true;
  int cnt = 0, gl = -1, a1 = -1, a2 = -1;
  int glseq[NR] = {}, a1seq[36] = {}, a2seq[27] = {}, extra[NS + 8] = {};
  {
    int n = 0;
    auto pre = [&](int k, int a, int b, int wgt) { P.pre[n++] = Op{k, a, b, 0}; cnt += wgt; return cnt; };
    pre(WAITW, 0, 0, 0);
    while (gl < 8 - 1) { ++gl; glseq[gl] = pre(GL, gl, 0, 1); }
    pre(WAITH, 0, 0, 0);
    while (a1 < alast(G ? 1 : 1 - 1)) { ++a1; a1seq[a1] = pre(A1, a1 >> 1, a1 & 1, 1); }
    P.npre = n;
  }
  for (int st = 0; st < NS; ++st) {
    if (gl < need_round(st) || (st < NS1 ? a1 : a2) < alast(st)) P.ok = false;
    int need = glseq[need_round(st)];
    need = extra[st] > need ? extra[st] : need;
    P.wait[st] = cnt - need > 63 ? 63 : cnt - need;
    Op ld[24] = {}; int nl = 0, fixed = 0;
    for (int i = 0; i < 9; ++i) if (s_sth(G, i) == st) fixed += WST;
    for (int tp = 0; tp < 8; ++tp) if (st == s_stp(G, tp)) fixed += WST;
    for (int i = 0; i < NLATE; ++i) if (st == S_MK[i] - G) fixed += WST;
    {
      int g2 = gl, b1 = a1, b2 = a2;
      const int dead = 9 * (st + G) - 133;
      const int done = st == 0 ? -1 : piece(9 * st - 1);
      for (int budget = CAP - fixed;; --budget) {
        const int BIG = 1000;
        int ng = (g2 < NR - 1 && 8 * (g2 + 1) + 7 - RINGP <= done && (g2 + 1 < 21 || st >= S_V)) ? gl_need(g2 + 1) : BIG;
        int na = BIG;
        if (b1 < 35) na = a_need(0, b1 + 1);
        else if (b2 < 26 && st >= s_chk(a2class(b2 + 1)) && b2 + 1 < (dead > 0 ? dead : 0) + 0) na = a_need(1, b2 + 1);
        int m = ng < na ? ng : na;
        if (m == BIG || (budget <= 0 && m - st > (b1 < 35 ? 1 : 2) + G)) break;
        if (na <= ng) { if (b1 < 35) { ++b1; ld[nl++] = Op{A1, b1 >> 1, b1 & 1, 0}; } else { ++b2; ld[nl++] = Op{A2, a2_kk(b2), a2_b(b2), 0}; } }
        else { ++g2; ld[nl++] = Op{GL, g2, 0, 0}; }
      }
    }
    int n = 0, cmid = 0;
    auto req = [&](int at, int seq) { if (extra[at] < seq) extra[at] = seq; };
    auto opp = [&](int pos, int k, int a, int b, int wgt) { P.ops[st][n++] = Op{k, a, b, pos}; cnt += wgt; return cnt; };
    auto op = [&](int k, int a, int b, int wgt) { return opp(G ? 9 : 0, k, a, b, wgt); };
    for (int c = 0; c < 4; ++c) if (st == s_chk(c) - 1) req(s_chk(c), opp(0, POLL, c, 0, 1));
    if (st == S_V - 3) req(S_V - 1, opp(0, POLLV, 0, 0, 1));
    if (st == S_QA - 2) req(S_QA, opp(0, FLL, 0, 0, 1));
    if (st == S_QB - 2) req(S_QB, opp(0, FLL, 2, 0, 1));
    if (st == S_CM - 2) req(S_CM, opp(0, FLL, 1, 0, 1));
    for (int i = 0; i <= TA; ++i) if (st == (i <= TB ? S_QB : S_QA)) P.qw[i] = opp(0, QL, i, 0, i ? 1 : 0);
    if (G) cmid = cnt;
    for (int tp = 0; tp < 8; ++tp) if (st == s_stp(G, tp)) { int q = op(STP, tp, 0, tp ? 1 : 0); if (tp <= TA) req(tp <= TB ? S_FB : S_FA, q); }
    for (int il = 0; il < nl; ++il) {
      int q = op(ld[il].k, ld[il].a, ld[il].b, 1);
      if (ld[il].k == GL) glseq[++gl] = q; else if (ld[il].k == A1) a1seq[++a1] = q; else a2seq[++a2] = q;
    }
    for (int h = 0; h < 9; ++h) if (s_sth(G, h) == st) req(s_pub(c_sth(h)), op(STH, h, 0, 1));
    for (int i = TA + 1; i < 8; ++i) if (st == s_ql(i)) P.qw[i] = op(QL, i, 0, 1);
    for (int i = 0; i < NLATE; ++i) if (st == S_MK[i] - G) req(S_FM, op(MARK, i, 0, 1));
    if (st == 32) { op(HRES, 0, 0, 1); P.qw[8] = op(HRES, 1, 0, 1); }
    if (st == 2 * WA + 1) op(VAR, 0, 0, 0);
    if (st == 32) op(VAR, 1, 0, 0);
    if (!G) cmid = cnt;
    for (int U = 9 * st; U < 9 * st + 9; ++U) if (kfirst(U) && (st < NS1 || a2_loaded(u_k(U)))) {
      int d = cmid - (st < NS1 ? a1seq[2 * u_k(U) + 1] : a2seq[a2_last(u_k(U))]); P.awt[U] = d > 63 ? 63 : d;
    }
    P.nops[st] = n;
    if (n > 24) P.ok = false;
  }
  for (int i = TA + 1; i < 8; ++i) if (s_ql(i) >= NS) P.qw[i] = ++cnt;
  for (int i = 0; i < 9; ++i) P.qw[i] = cnt - P.qw[i];
  return P;
}
constexpr Prog PGS[2] = {build(0), build(1)};
static_assert(PGS[0].ok && PGS[1].ok, "schedule: an operand is needed before it is requested");
static_assert(NLATE <= 3 && S_FM + 3 < S_CM && S_QA < NS, "marker steps");
static_assert(TA >= 2 && TA <= 5 && S_FA < NS - 1 && s_stp(0, 7) < NS - 1 && 9 * s_sth(0, 8) <= N1 + p2_unit(3, 0), "tail steps");
constexpr int first_op(int G, int st, int i) { int n = 0; while (n < PGS[G].nops[st] && PGS[G].ops[st][n].pos < i) ++n; return n; }
}

static constexpr int L_FFN = sch::RINGP * 1024;
static constexpr int L_ALL = L_FFN + 256;
static constexpr int L_Q9 = 32768;

static constexpr int KF_CP = 4160, KF_CH = 4 * KF_CP;
static constexpr int L_KF = 0, L_KV = 4 * KF_CH, L_RED = L_KV + 18 * 1024, L_Z = L_RED;
static constexpr int L_XS = 98304, L_XI = L_XS + 256;
static_assert(L_XI + 2 * 20736 <= L_FFN, "LDS");
static constexpr int L_STG = L_RED, STGP = 1088, STGW = 5 * STGP;
static_assert(L_STG + 8 * STGW <= L_FFN && L_Z + 8 * KF_CP <= L_FFN && 8 * 12288 + 256 <= L_FFN, "LDS");

extern "C" __global__ __launch_bounds__(512, 2) void mega(const char* __restrict__ xg, const char* __restrict__ kg, const char* __restrict__ vg,
                                                       const char* __restrict__ skg, const char* __restrict__ kfg, const unsigned* __restrict__ ssmD,
                                                       const char* __restrict__ w1g, const char* __restrict__ w2g, char* __restrict__ ws,
                                                       char* __restrict__ out) {
  using namespace sch;
  extern __shared__ __attribute__((aligned(16))) char sm[];
  asm volatile("" ::"s"(xg), "s"(kg), "s"(vg), "s"(skg), "s"(kfg), "s"(ssmD), "s"(w1g), "s"(w2g), "s"(ws), "s"(out));
  const int tid = threadIdx.x, lane = tid & 63;
  const int w = __builtin_amdgcn_readfirstlane(tid >> 6);
  const unsigned lbase = (unsigned)(size_t)(LAS char*)sm;
  const int g = lane >> 4, c = lane & 15;
  const unsigned l16 = lane * 16;
  const v4u srd = mksrd(ws, OFF_ST);
  unsigned tag, fwv;
  const int bx = blockIdx.x & 7, bi = blockIdx.x >> 3;
  const int mt8 = (bx >> 2) * 4 + (bi >> 3), j = 8 * (bx & 3) + (bi & 7);
  const unsigned fwo = (((min(lane, 11) >> 1) * 144 + ((9 * j) >> 1) + (min(lane, 11) & 1) * 4) >> 3) * 128;

  if (__builtin_expect(blockIdx.x < 144, 1)) {
    const int cg = 18 * (blockIdx.x & 7) + (blockIdx.x >> 3), sg = cg >> 4, rb = cg & 15, ch = w & 3, eh = w >> 2;
    unsigned epv;
    unsigned stg = lbase + L_STG + w * STGW;
    const char* stgp = sm + L_STG + w * STGW;
    {
      const unsigned vo = (128 * rb + 16 * w - 8 + (lane >> 3)) * ROWB + sg * 128 + (lane & 7) * 16;
      const v4u sk_ = mksrd(kg, S * ROWB), sv_ = mksrd(vg, S * ROWB);
      blds16<0, STGP>(sk_, vo + 8 * ROWB, 0u, stg);
      blds16<0, 2 * STGP>(sk_, vo + 16 * ROWB, 0u, stg);
      blds16<0, 0>(sk_, vo, 0u, stg);
      blds16<0, 3 * STGP>(sv_, vo + 8 * ROWB, 0u, stg);
      blds16<0, 4 * STGP>(sv_, vo + 16 * ROWB, 0u, stg);
    }
    if (w == 7 && blockIdx.x < 96) {
      unsigned lo, hi, len;
      asm volatile("s_getpc_b64 vcc\n\ts_add_u32 %0, vcc_lo, mega@rel32@lo+4\n\ts_addc_u32 %1, vcc_hi, mega@rel32@hi+12\n\ts_mov_b32 %2, mega_end-mega"
                   : "=s"(lo), "=s"(hi), "=s"(len)::"scc", "vcc");
      blds4(v4u{lo, hi & 0xffffu, len > 0x40000u ? 0u : len, 0x00020000u}, ((blockIdx.x >> 3) * 64 + lane) * 128, lbase + L_FFN);
    }
    bl4c<OFF_EP>(epv, srd, 0u, 0u);
    const int cgl = lane >> 2, tcl = (lane >> 1) & 1, cpl = lane & 1;
    v4u skw, kw[2];
    bl16<0>(skw, mksrd(skg, D * 8), (64 * sg + 4 * cgl + 2 * cpl) * 8, 0u);
    const v2u kmid = *(const v2u*)(kfg + (size_t)(4 * cg + ch) * 4096 + 2048);
    const unsigned dpair = ssmD[2 * cg + (ch >> 1)];
    if (tid < 128) *(v4u*)(sm + L_KV + tid * 16) = v4u{0u, 0u, 0u, 0u};
    asm volatile("s_waitcnt vmcnt(0)" : "+v"(skw), "+v"(epv)::"memory");
    const v4u skf = mksrd(kfg, S * D * 2);
    bl16<0>(kw[0], skf, l16, (unsigned)((4 * cg + ch) * 4096 + eh * 2048));
    bl16<1024>(kw[1], skf, l16, (unsigned)((4 * cg + ch) * 4096 + eh * 2048));
    tag = __builtin_amdgcn_readfirstlane(epv) + 1u;
    {
      const char* p = stgp + tcl * STGP + (2 * cgl + cpl) * 4;
      f32x2 kf[11], r[8];
#pragma unroll
      for (int i = 0; i < 11; ++i) kf[i] = up2(*(const unsigned*)(p + ((5 + i) >> 3) * STGP + ((5 + i) & 7) * 128));
      const f32x2 s0 = {lo2f(skw.x), lo2f(skw.z)}, s1 = {hi2f(skw.x), hi2f(skw.z)}, s2 = {lo2f(skw.y), lo2f(skw.w)}, s3 = {hi2f(skw.y), hi2f(skw.w)};
#pragma unroll
      for (int i = 0; i < 8; ++i)
        r[i] = fma2(kf[i + 3], s0, fma2(kf[i + 2], s1, fma2(kf[i + 1], s2, kf[i] * s3))) * up2(*(const unsigned*)(p + 3 * STGP + i * 128));
      unsigned so = OFF_KV + sg * 262144 + (8 * rb + w) * 128;
      unsigned o0[4] = {pk(r[7].x, r[6].x), pk(r[5].x, r[4].x), pk(r[3].x, r[2].x), pk(r[1].x, r[0].x)};
      unsigned o1[4] = {pk(r[7].y, r[6].y), pk(r[5].y, r[4].y), pk(r[3].y, r[2].y), pk(r[1].y, r[0].y)};
#pragma unroll
      for (int i = 0; i < 4; ++i) {
        const int a = o0[i], b = o1[i];
        o0[i] = __builtin_amdgcn_update_dpp(a, b, 0x114, 0xf, 0xa, false);
        o1[i] = __builtin_amdgcn_update_dpp(b, a, 0x104, 0xf, 0x5, false);
      }
      bs16c<0>(v4u{o0[0], o0[1], o0[2], o0[3]}, srd, (lane >> 3) * 32768 + (lane & 7) * 16, so);
      bs16c<16384>(v4u{o1[0], o1[1], o1[2], o1[3]}, srd, (lane >> 3) * 32768 + (lane & 7) * 16, so);
    }
    asm volatile("s_waitcnt vmcnt(2)" : "+v"(kw[0]), "+v"(kw[1])::"memory");
    {
      auto put = [&](v4u a, v2u n, int X) {
        unsigned a01 = __builtin_amdgcn_alignbit(a.y, a.x, 16), a12 = __builtin_amdgcn_alignbit(a.z, a.y, 16), a23 = __builtin_amdgcn_alignbit(a.w, a.z, 16);
        unsigned a3n = __builtin_amdgcn_alignbit(n.x, a.w, 16), ann = __builtin_amdgcn_alignbit(n.y, n.x, 16);
        char* p = sm + L_KF + ch * KF_CH + X * 16;
        *(v4u*)(p) = a;
        *(v4u*)(p + KF_CP) = v4u{a01, a12, a23, a3n};
        *(v4u*)(p + 2 * KF_CP) = v4u{a.y, a.z, a.w, n.x};
        *(v4u*)(p + 3 * KF_CP) = v4u{a12, a23, a3n, ann};
      };
      unsigned f0 = __builtin_amdgcn_readlane(kw[1].x, 0), f1 = __builtin_amdgcn_readlane(kw[1].y, 0);
      v2u n0 = {(unsigned)__builtin_amdgcn_update_dpp((int)f0, (int)kw[0].x, 0x130, 0xf, 0xf, false),
                (unsigned)__builtin_amdgcn_update_dpp((int)f1, (int)kw[0].y, 0x130, 0xf, 0xf, false)};
      put(kw[0], n0, 128 * eh + lane + 2);
      asm volatile("s_waitcnt vmcnt(0)\n\ts_barrier" ::: "memory");
      if (w == 0) set_flag(srd, OFF_F0 + cg * 128, tag, lane);
      v2u n1 = {(unsigned)__builtin_amdgcn_update_dpp((int)kmid.x, (int)kw[1].x, 0x130, 0xf, 0xf, false),
                (unsigned)__builtin_amdgcn_update_dpp((int)kmid.y, (int)kw[1].y, 0x130, 0xf, 0xf, false)};
      put(kw[1], n1, 128 * eh + 64 + lane + 2);
      if (eh == 0) {
        v2u k0 = {(unsigned)__builtin_amdgcn_readlane(kw[0].x, 0), (unsigned)__builtin_amdgcn_readlane(kw[0].y, 0)};
        if (lane < 2) put(v4u{0u, 0u, 0u, 0u}, lane == 1 ? k0 : v2u{0u, 0u}, lane);
      }
    }
    asm volatile("s_waitcnt lgkmcnt(0)\n\ts_barrier" ::: "memory");
    const unsigned abase = lbase + L_KF + ch * KF_CH + ((1 + c) & 3) * KF_CP + (4 * (g >> 1) - 2 * (g & 1) + 2 + ((1 + c) >> 2)) * 8 + eh * 256;
    v2u Al[2][8], Ah[2][8];
    v4u B[2][8];
    unsigned baddr[4];
#pragma unroll
    for (int e = 0; e < 4; ++e) { int x0 = 32 + 2 * c - 2 * (g >> 1) + (g & 1) - 4 * (4 * eh + e); baddr[e] = lbase + L_KV + x0 * 64 + ((ch + (x0 >> 2)) & 3) * 16; }
    auto rdA = [&](auto E, auto I) { constexpr int e = decltype(E)::v, i = decltype(I)::v; lds_rd64(Al[e & 1][i], abase, 64 * e + 512 * i); lds_rd64(Ah[e & 1][i], abase, 64 * e + 512 * i + 8); };
    auto rdB = [&](auto E, auto I) { constexpr int e = decltype(E)::v, i = decltype(I)::v; lds_rd128(B[e & 1][i], baddr[e], 2048 * i); };
    sfor<0, 8>([&](auto I) { rdA(IC<0>{}, I); });
    v4u AD;
    {
      const int jj = 7 + 8 * (g & 1) - c;
      const unsigned dv = (eh == 0 && g < 2 && jj >= 0 && jj < 8) ? ((ch & 1) ? dpair >> 16 : dpair & 0xffffu) << (16 * (jj & 1)) : 0u;
      AD = v4u{(jj >> 1) == 0 ? dv : 0u, (jj >> 1) == 1 ? dv : 0u, (jj >> 1) == 2 ? dv : 0u, (jj >> 1) == 3 ? dv : 0u};
      pin(AD);
    }
    wait_flags<2>(srd, OFF_F0 + (sg * 16 + 2 * w) * 128, tag, lane);
    {
      v4u kv[2];
      unsigned so = OFF_KV + cg * 16384 + w * 2048;
      bl16c<0>(kv[0], srd, l16, so);
      bl16c<1024>(kv[1], srd, l16, so);
      const int tc = 32 * w + 2 * (lane >> 3) + ((lane >> 1) & 1), cc = 2 * (lane & 1) + ((lane >> 2) & 1);
      char* p = sm + L_KV + (tc + 32) * 64 + ((cc + (tc >> 2)) & 3) * 16;
      asm volatile("s_waitcnt vmcnt(0)" : "+v"(kv[0]), "+v"(kv[1])::"memory");
      *(v4u*)p = kv[0];
      *(v4u*)(p + 1024) = kv[1];
    }
    asm volatile("s_waitcnt lgkmcnt(0)\n\ts_barrier"
                 : "+v"(Al[0][0]), "+v"(Al[0][1]), "+v"(Al[0][2]), "+v"(Al[0][3]), "+v"(Al[0][4]), "+v"(Al[0][5]), "+v"(Al[0][6]), "+v"(Al[0][7]),
                   "+v"(Ah[0][0]), "+v"(Ah[0][1]), "+v"(Ah[0][2]), "+v"(Ah[0][3]), "+v"(Ah[0][4]), "+v"(Ah[0][5]), "+v"(Ah[0][6]), "+v"(Ah[0][7])::"memory");
    sfor<0, 8>([&](auto I) { rdB(IC<0>{}, I); });
    f32x4 acc[8];
#pragma unroll
    for (int i = 0; i < 8; ++i) acc[i] = f32x4{0.f, 0.f, 0.f, 0.f};
    v4u xr[2], st1[2];
    f32x2 xf[4][2], qf[4][2];
    auto frA = [&](int s, int a) { return v4u{Al[s][a].x, Al[s][a].y, Ah[s][a].x, Ah[s][a].y}; };
    sfor<0, 4>([&](auto E) {
      constexpr int e = decltype(E)::v, s = e & 1;
      if constexpr (e > 0) {
        asm volatile("s_waitcnt lgkmcnt(0)"
                     : "+v"(Al[s][0]), "+v"(Al[s][1]), "+v"(Al[s][2]), "+v"(Al[s][3]), "+v"(Al[s][4]), "+v"(Al[s][5]), "+v"(Al[s][6]), "+v"(Al[s][7]),
                       "+v"(Ah[s][0]), "+v"(Ah[s][1]), "+v"(Ah[s][2]), "+v"(Ah[s][3]), "+v"(Ah[s][4]), "+v"(Ah[s][5]), "+v"(Ah[s][6]), "+v"(Ah[s][7]),
                       "+v"(B[s][0]), "+v"(B[s][1]), "+v"(B[s][2]), "+v"(B[s][3]), "+v"(B[s][4]), "+v"(B[s][5]), "+v"(B[s][6]), "+v"(B[s][7])::"memory");
      }
      __builtin_amdgcn_sched_barrier(0);
      if constexpr (e < 3) {
        sfor<0, 8>([&](auto BI) {
          constexpr int b = decltype(BI)::v;
          if constexpr (e == 0 && b < 2) asm volatile("s_waitcnt lgkmcnt(%1)" : "+v"(B[s][b]) : "i"(7 - b) : "memory");
          if constexpr (e == 0 && b == 2) asm volatile("s_waitcnt lgkmcnt(0)" : "+v"(B[s][2]), "+v"(B[s][3]), "+v"(B[s][4]), "+v"(B[s][5]), "+v"(B[s][6]), "+v"(B[s][7])::"memory");
          sfor<0, 8 - b>([&](auto AI) {
            constexpr int a = decltype(AI)::v, b0 = e == 0 ? 2 : 0, bb = b - b0;
            acc[a + b] = mfma(frA(s, a), B[s][b], acc[a + b]);
            pin(acc[a + b]);
            constexpr int per = e == 0 ? 3 : 2;
            if constexpr (bb >= 0 && a < 2 * per && per * bb + (a >> 1) < 8 && !(e == 0 && b == 3 && a == 5)) {
              if constexpr (a & 1) rdB(IC<e + 1>{}, IC<per * bb + (a >> 1)>{});
              else rdA(IC<e + 1>{}, IC<per * bb + (a >> 1)>{});
            }
          });
          if constexpr (e == 0) { acc[b] = mfma(AD, B[s][b], acc[b]); pin(acc[b]); if constexpr (b == 3) rdB(IC<1>{}, IC<5>{}); }
          if constexpr (e == 2 - 1 && b == 4) {
            bl16c<OFF_ST1>(st1[0], srd, l16, (unsigned)(w * 2048));
            bl16c<OFF_ST1 + 1024>(st1[1], srd, l16, (unsigned)(w * 2048));
          }
          if constexpr (e == 2 && b == 4) {
            asm volatile("s_waitcnt vmcnt(0)" : "+v"(st1[0]), "+v"(st1[1])::"memory");
            wait_stats(st1[0], srd, l16, OFF_ST1 + w * 2048, tag);
            wait_stats(st1[1], srd, l16, OFF_ST1 + w * 2048 + 1024, tag);
            unsigned xs = OFF_XT + cg * 16384 + w * 2048;
            bl16c<0>(xr[0], srd, l16, xs);
            bl16c<1024>(xr[1], srd, l16, xs);
          }
          __builtin_amdgcn_sched_barrier(0);
        });
      } else {
        sfor<0, 9>([&](auto CI) {
          constexpr int ct = decltype(CI)::v;
          if constexpr (ct < 8) sfor<0, ct + 1>([&](auto AI) { constexpr int a = decltype(AI)::v; acc[ct] = mfma(frA(s, a), B[s][ct - a], acc[ct]); pin(acc[ct]); });
          if constexpr (ct > 0) *(v2u*)(sm + L_Z + w * KF_CP + (256 * (ct - 1) + 16 * c + 4 * g) * 2) = v2u{pk(acc[ct - 1][0], acc[ct - 1][1]), pk(acc[ct - 1][2], acc[ct - 1][3])};
          __builtin_amdgcn_sched_barrier(0);
        });
      }
    });
    asm volatile("s_waitcnt vmcnt(0)" : "+v"(xr[0]), "+v"(xr[1])::"memory");
    {
      const unsigned sv[4] = {st1[0].x, st1[0].z, st1[1].x, st1[1].z};
      const unsigned xw[8] = {xr[0].x, xr[0].y, xr[0].z, xr[0].w, xr[1].x, xr[1].y, xr[1].z, xr[1].w};
#pragma unroll
      for (int i = 0; i < 4; ++i) {
        const float rs = __builtin_amdgcn_rcpf(hi2f(sv[i])), nm = -lo2f(sv[i]) * rs;
#pragma unroll
        for (int k = 0; k < 2; ++k) { xf[i][k] = up2(xw[2 * i + k]); qf[i][k] = fma2(xf[i][k], f32x2{rs, rs}, f32x2{nm, nm}); pin(qf[i][k]); }
      }
    }
    asm volatile("s_waitcnt lgkmcnt(0)\n\ts_barrier" ::: "memory");
    sfor<0, 2>([&](auto IP) {
      constexpr int ip = decltype(IP)::v;
      unsigned h[4];
#pragma unroll
      for (int k = 0; k < 2; ++k) {
        const char* p = sm + L_Z + (c >> 2) * KF_CP + (256 * w + 128 * ip + 32 * g + 16 * k + 4 * (c & 3)) * 2;
        const v2u z0 = __builtin_bit_cast(v2u, __builtin_amdgcn_ds_read_tr16_b64_v4i16((LAS s16x4*)p));
        const v2u z1 = __builtin_bit_cast(v2u, __builtin_amdgcn_ds_read_tr16_b64_v4i16((LAS s16x4*)(p + 4 * KF_CP)));
        h[2 * k] = pk2(fma2(qf[2 * ip + k][0], up2(z0.x) + up2(z1.x), xf[2 * ip + k][0]));
        h[2 * k + 1] = pk2(fma2(qf[2 * ip + k][1], up2(z0.y) + up2(z1.y), xf[2 * ip + k][1]));
      }
      bs16c<OFF_H + ip * 4 * 36864>(v4u{h[0], h[1], h[2], h[3]}, srd, g * 36864 + c * 16, (unsigned)(w * 8 * 36864 + cg * 256));
    });
    unsigned f_;
    bl4c<OFF_FW>(f_, srd, fwo, 0u);
    asm volatile("s_waitcnt vmcnt(1)" ::: "memory");
    set_flag(srd, OFF_FH + (w * 144 + cg) * 128, tag, lane);
    asm volatile("s_waitcnt vmcnt(1)" : "+v"(f_)::"memory");
    fwv = f_;
  } else {
    const int ri = blockIdx.x - 144;
    unsigned epv;
    blds4c(mksrd(ws, (w == 0 && ri < 16) ? OFF_ST : 0), lane * 0x100000 + (ri >> 3) * 0x80000, 0u, lbase + L_FFN);
    bl4c<OFF_EP>(epv, srd, 0u, 0u);
    const int sl = w >= 4 ? ri : 112 + ri;
    const bool dostat = sl < 128;
    const int srow = (sl >> 1) * 32 + (sl & 1) * 8 + 2 * (w & 3) + (g >> 1) + 16 * (g & 1);
    v2u xr[9];
    {
      const v4u sx_ = mksrd(xg, dostat ? S * ROWB : 0);
      if (!dostat) __builtin_amdgcn_s_sleep(8);
      sfor<0, 9>([&](auto I) { bl8<decltype(I)::v * 128>(xr[decltype(I)::v], sx_, srow * ROWB + c * 8, 0u); });
    }
    const int u = ri * 8 + w;
    const int kb1 = u / 144, bk1 = u % 144, kb2 = u / 18, bk2 = u % 18;
    const v4u srdo = mksrd(ws, ri < 108 ? OFF_ST : 0);
    unsigned lo = lbase + w * 6144;
    const v4u sw1 = mksrd(w1g, ri < 108 ? D * F * 2 : 0);
    unsigned so1 = (kb1 * 96 + (w & 1) * 48) * (F * 2) + (bk1 >> 1) * 128;
    auto ld1 = [&](auto P) { blds16<decltype(P)::v * 8 * F * 2, decltype(P)::v * 1024>(sw1, (lane >> 3) * (F * 2) + (lane & 7) * 16, so1, lo); };
    const v4u sw2 = mksrd(w2g, ri < 108 ? D * F * 2 : 0);
    unsigned so2 = (kb2 * 96 + (w & 1) * 48) * ROWB + (bk2 >> 1) * 128;
    auto ld2 = [&](auto P) { blds16<decltype(P)::v * 8 * ROWB, 49152 + decltype(P)::v * 1024>(sw2, (lane >> 3) * ROWB + (lane & 7) * 16, so2, lo); };
    asm volatile("s_barrier" ::: "memory");
    sfor<0, 3>(ld1);
    asm volatile("s_waitcnt vmcnt(%1)" : "+v"(epv) : "i"(3) : "memory");
    sfor<3, 6>(ld1);
    tag = __builtin_amdgcn_readfirstlane(epv) + 1u;
    {
#pragma unroll
      for (int i = 0; i < 9; ++i) pin(xr[i]);
      float s = 0.f, s2 = 0.f;
#pragma unroll
      for (int i = 0; i < 9; ++i) { s = sum2(xr[i].x, s); s2 = sum2(xr[i].y, s2); }
      const float mean = row16_sum(s + s2) * (1.0f / D);
      f32x2 q = {0.f, 0.f}, q2 = {0.f, 0.f};
      const f32x2 mm = {mean, mean};
#pragma unroll
      for (int i = 0; i < 9; ++i) { f32x2 d0 = up2(xr[i].x) - mm, d1 = up2(xr[i].y) - mm; q += d0 * d0; q2 += d1 * d1; }
      q += q2;
      const float var = rbf(row16_sum(q.x + q.y) * (1.0f / D));
      if (c == 0) *(v2u*)(sm + L_XS + w * 32 + g * 8) = v2u{pk(mean, __builtin_amdgcn_sqrtf(rbf(var + 1e-5f))), tag};
#pragma unroll
      for (int i = 0; i < 9; ++i) *(v2u*)(sm + L_XI + (w >> 2) * 20736 + (c + 16 * i) * 144 + (w & 3) * 32 + g * 8) = xr[i];
    }
    asm volatile("s_waitcnt lgkmcnt(0)\n\ts_barrier" ::: "memory");
#pragma nounroll
    for (int un = 1; un >= (ri < 16 ? 0 : 1); --un) {
      const int ul = un ? ri : 112 + ri;
#pragma unroll
      for (int r = 0; r < 3; ++r) {
        const int id = tid + 512 * r;
        if (r < 2 || w < 2) bs16c<OFF_XT>(*(const v4u*)(sm + L_XI + un * 20736 + (id >> 3) * 144 + (id & 7) * 16), srd, (id >> 3) * 16384 + (id & 7) * 16, (unsigned)(ul * 128));
      }
    }
    sfor<0, 3>(ld2);
    const char* wl = sm + (w >> 1) * 12288 + (4 * g + (c >> 2)) * 128 + (w & 1) * 64 + (c & 3) * 8;
    auto frag = [&](const char* p) {
      v2u a = __builtin_bit_cast(v2u, __builtin_amdgcn_ds_read_tr16_b64_v4i16((LAS s16x4*)p));
      v2u b = __builtin_bit_cast(v2u, __builtin_amdgcn_ds_read_tr16_b64_v4i16((LAS s16x4*)(p + 2048)));
      return v4u{a.x, a.y, b.x, b.y};
    };
    unsigned dst;
    {
      const int l7 = lane & 7, i = l7 >= 3, un = l7 - 3 * i;
      const int r1 = bk1 % 9, own = 2 * (bk1 / 9) + ((r1 + i + 3) >> 3), tau1 = r1 == 4 ? 8 : 2 * (r1 > 4 ? r1 - 5 : r1) + i;
      const unsigned d1 = OFF_W1 + (own * 172 + p1_unit(lane >= 16 ? 18 : 3 * kb1 + un, tau1)) * 1024;
      const int T = 2 * bk2 + i, nt = T / 9, tau = T - 9 * nt, kst = 3 * kb2 + un, s8 = kst / 18, kl = kst - 18 * s8, hi = kl >= 9, r = kl - 9 * hi;
      const int kk = r == 4 ? 7 + (hi ^ (nt >> 1)) : p2_kk((2 * hi + (r > 4) - nt) & 3, r > 4 ? r - 5 : r);
      const int tp = tau == 8 ? 0 : tau == s8 ? 8 : 1 + ((tau - s8 - 1) & 7);
      const unsigned d2 = OFF_W2 + ((s8 * 4 + nt) * 162 + p2_unit(kk, tp)) * 1024 + ((r == 4 && (nt & 1)) ? 1 : 0);
      dst = (lane & 8) ? d2 : d1;
      pin(dst);
    }
    asm volatile("s_waitcnt vmcnt(%0)\n\ts_barrier" ::"i"(3) : "memory");
    if (w == 0) bs16c<OFF_ST1>(*(const v4u*)(sm + L_XS + (lane & 15) * 16), srd, lane < 8 ? (ri < 16 ? (112 + ri) * 128 + l16 : 0x80000000u) : lane < 16 ? ri * 128 + l16 - 128 : 0x80000000u, 0u);
    sfor<3, 6>(ld2);
    {
      float cs[2] = {0.f, 0.f};
#pragma unroll
      for (int i = 0; i < 2; ++i) {
#pragma unroll
        for (int un = 0; un < 3; ++un) {
          const unsigned so = __builtin_amdgcn_readlane(dst, 3 * i + un);
          v4u f = frag(wl + un * 4096 + i * 32);
          cs[i] = sum2(f.w, sum2(f.z, sum2(f.y, sum2(f.x, cs[i]))));
          bs16c<64>(f, srdo, l16, so - 64);
        }
        cs[i] += __shfl_xor(cs[i], 16); cs[i] += __shfl_xor(cs[i], 32);
      }
      const float v = g == 0 ? cs[0] : g == 1 ? cs[1] : 0.f;
      const float h1 = rbf(v), h2 = rbf(v - h1);
      const unsigned f0 = __builtin_amdgcn_readlane(dst, 16), f1 = __builtin_amdgcn_readlane(dst, 19);
      unsigned vo = ((g & 1) ? f1 : f0) + (g < 2 ? ((kb1 >> 1) * 16 + c) * 16 + (kb1 & 1) * 8 : (48 + c) * 16 + kb1 * 8);
      vo = (g >= 2 && kb1 >= 2) ? 0x80000000u : vo;
      bs8c<0>(v2u{pk(h1, h2), pk(v - h1 - h2, 0.f)}, srdo, vo, 0u);
    }
    asm volatile("s_waitcnt vmcnt(7)\n\ts_barrier" ::: "memory");
#pragma unroll
    for (int i = 0; i < 2; ++i) {
#pragma unroll
      for (int un = 0; un < 3; ++un) {
        const unsigned so = __builtin_amdgcn_readlane(dst, 8 + 3 * i + un);
        const v4u f = frag(wl + 49152 + un * 4096 + i * 32);
        const bool sw = so & 1;
        bs16c<64>(v4u{sw ? f.z : f.x, sw ? f.w : f.y, sw ? f.x : f.z, sw ? f.y : f.w}, srdo, l16, (so & ~1u) - 64);
      }
      if (i == 0) {
        asm volatile("s_waitcnt vmcnt(3)\n\ts_barrier" ::: "memory");
        if (w == 0) set_flag(srd, OFF_FW + ri * 128, tag, lane);
      }
    }
    unsigned f_;
    bl4c<OFF_FW>(f_, srd, fwo, 0u);
    asm volatile("s_waitcnt vmcnt(0) lgkmcnt(0)\n\ts_barrier" : "+v"(f_)::"memory");
    fwv = f_;
    if (w == 0) set_flag(srd, OFF_FV + ri * 128, tag, lane);
  }

  const int s8 = j >> 2, nt = j & 3, blk = 8 * mt8 + w;
  unsigned w1src = OFF_W1 + j * 172 * 1024 + w * 1024;
  unsigned w2src = OFF_W2 + j * 162 * 1024 - 172 * 1024 + w * 1024;
  unsigned a1src = OFF_H + blk * 36864;
  unsigned hsrc[4];
#pragma unroll
  for (int d = 0; d < 4; ++d) hsrc[d] = OFF_HID + (blk * 32 + 4 * s8 + ((nt + d) & 3)) * 9216;
  unsigned cnt = OFF_FC + (mt8 * 32 + 4 * s8) * 128;
  const unsigned inb = OFF_IN + (mt8 * 4 + nt) * 8 * INB;
  unsigned wf = OFF_FP + (mt8 * 4 + nt) * 1024;

  unsigned ldsw = lbase + w * 1024;
  bool early_ok = true;

  v4u A1r[19][2], A2r[18][2], Bq[LA + 1], q[8], hres[2];
  unsigned pollv[4], fl, flx[3];
  f32x4 acc[9][2], acc2[9][2], sum[2], gram[2];
  float mean[2], rs[2];
  sum[0] = sum[1] = gram[0] = gram[1] = f32x4{0.f, 0.f, 0.f, 0.f};
  const v4u ones = {0x3f803f80u, 0x3f803f80u, 0x3f803f80u, 0x3f803f80u};
#pragma unroll
  for (int i = 0; i < 9; ++i) { acc[i][0] = acc[i][1] = acc2[i][0] = acc2[i][1] = f32x4{0.f, 0.f, 0.f, 0.f}; }
  unsigned bl[3] = {lbase + l16, lbase + l16 + 65536, lbase + l16 + 131072};
  pin(bl[0]); pin(bl[1]); pin(bl[2]);

  auto LN = [&]() { unsigned t = l16; asm volatile("" : "+v"(t)); return (int)(t >> 4); };
  auto relu8 = [&](f32x4 a, f32x4 b) { return v4u{relu2(pk(a[0], a[1])), relu2(pk(a[2], a[3])), relu2(pk(b[0], b[1])), relu2(pk(b[2], b[3]))}; };
  auto doop = [&](auto K, auto AA, auto BB) {
    constexpr int k = decltype(K)::v, a = decltype(AA)::v, b = decltype(BB)::v;
    if constexpr (k == WAITH) {
      if (blockIdx.x >= 144) {
        if (w == 0) wait_flags_v(srd, OFF_FH + mt8 * 144 * 128, LN() < 8 ? (LN() * 18 + 17) * 128 : 0x80000000u, tag);
        asm volatile("s_barrier" ::: "memory");
      }
      wait_flags<18>(srd, OFF_FH + (mt8 * 144 + w * 18) * 128, tag, LN());
      asm volatile("s_barrier" ::: "memory");
    }
    if constexpr (k == WAITW) {
      if (!__all(fwv == tag)) wait_flags_v(srd, OFF_FW, fwo, tag);
      asm volatile("s_waitcnt lgkmcnt(0)\n\ts_barrier" ::: "memory");
    }
    if constexpr (k == VAR) {
      const int ln = LN();
#pragma unroll
      for (int m = 0; m < 2; ++m) {
        if constexpr (a == 0) {
          const int r = ln & 3;
          const float sq = r == 0 ? gram[m][0] : r == 1 ? gram[m][1] : r == 2 ? gram[m][2] : gram[m][3];
          rs[m] = sq * (1.0f / D) - mean[m] * mean[m];
        } else {
          const float var = rbf(fmaxf(__shfl(rs[m], ((ln & 15) >> 2) * 16 + (ln & 15)), 0.f));
          rs[m] = __builtin_amdgcn_rcpf(rbf(__builtin_amdgcn_sqrtf(rbf(var + 1e-5f))));
        }
        pin(rs[m]);
      }
    }
    if constexpr (k == GL) {
      if constexpr (a == 21) w1src = w < 4 ? w1src : w2src;
      blds16c<a * 8192, ((8 * a) % RINGP) * 1024>(srd, l16, a <= 21 ? w1src : w2src, ldsw);
    }
    if constexpr (k == A1) bl16c<(2 * a + b) * 1024>(A1r[a][b], srd, l16, a1src);
    if constexpr (k == A2) {
      if constexpr (a == 7 || a == 8) bl16c<8192>(A2r[a][b], srd, l16, hsrc[0] + ((nt ^ (2 * (a - 7) + b)) - nt) * 9216);
      else { constexpr int p = a < 7 ? 0 : (a - 6) / 3, d = a < 7 ? a - 3 : a - 5 - 3 * p; bl16c<(2 * p + b) * 1024>(A2r[a][b], srd, l16, hsrc[d]); }
    }
    if constexpr (k == HRES) bl16c<0>(hres[a], srd, l16, a1src + (nt * 36 + (a ? 32 : 4 * s8)) * 256);
    if constexpr (k == STH) {
      if constexpr (a == 2) { A2r[7][0] = relu8(acc[8][0], acc[8][1]); bs16c<8192>(A2r[7][0], srd, l16, hsrc[0]); }
      else {
        constexpr int i = a < 2 ? a : a - 1, m = i & 1, p = i >> 1;
        A2r[p2_kk(0, p)][m] = relu8(acc[2 * p][m], acc[2 * p + 1][m]);
        bs16c<(2 * p + m) * 1024>(A2r[p2_kk(0, p)][m], srd, l16, hsrc[0]);
      }
    }
    if constexpr (k == POLL) bl4c<a * 32768>(pollv[a], srd, (LN() & 3) * 128, cnt);
    if constexpr (k == POLLV) bl4c<OFF_FV>(fl, srd, (lane < 14 ? lane : 0) * 128, (unsigned)(w * 14 * 128));
    if constexpr (k == FLL) bl4c<a * 32768>(flx[a], srd, (LN() & 7) * 128, wf);
    if constexpr (k == MARK) bs16c<(TA + a) * 8192>(v4u{~0u, ~0u, ~0u, ~0u}, srd, l16, inb + s8 * INB + w * 1024);
    if constexpr (k == QL) {
      if constexpr (a > 0) bl16c<(a - 1) * 8192>(q[a], srd, l16, inb + s8 * INB + w * 1024);
      else if (w == s8) {
        unsigned so = inb + s8 * INB, lo = lbase;
        sfor<0, 7>([&](auto I) { blds16c<57344 + decltype(I)::v * 1024, L_Q9 + decltype(I)::v * 1024>(srd, l16, so, lo); });
      }
    }
    if constexpr (k == STP) {
      v4u v = {pk(acc2[a][0][0], acc2[a][0][1]), pk(acc2[a][0][2], acc2[a][0][3]), pk(acc2[a][1][0], acc2[a][1][1]), pk(acc2[a][1][2], acc2[a][1][3])};
      if constexpr (a > TA) v = __builtin_elementwise_min(v, v4u{~1u, ~1u, ~1u, ~1u});
      if constexpr (a > 0) bs16c<(a - 1) * 8192>(v, srd, l16, inb + ((s8 + a) & 7) * INB + w * 1024);
      else { if (w != s8) bs16c<57344>(v, srd, l16, inb + w * INB + ((w - s8 - 1) & 7) * 1024); }
    }
  };
  auto rdB = [&](auto UU) {
    constexpr int U = decltype(UU)::v;
    constexpr int off = (piece(U) % RINGP) * 1024;
    if constexpr (U < NU) Bq[U % (LA + 1)] = *(const LAS v4u*)(size_t)(bl[off >> 16] + (off & 65535));
  };
  auto addq = [&](f32x4 (&o)[2], v4u v) {
    o[0] += f32x4{lo2f(v.x), hi2f(v.x), lo2f(v.y), hi2f(v.y)}; o[1] += f32x4{lo2f(v.z), hi2f(v.z), lo2f(v.w), hi2f(v.w)};
  };
  auto fin = [&](f32x4 (&o)[2], v4u hr) {
    const f32x2 r0 = {rs[0], rs[0]}, r1 = {rs[1], rs[1]};
    return v4u{pk2(fma2(f32x2{o[0][0], o[0][1]}, r0, up2(hr.x))), pk2(fma2(f32x2{o[0][2], o[0][3]}, r0, up2(hr.y))),
               pk2(fma2(f32x2{o[1][0], o[1][1]}, r1, up2(hr.z))), pk2(fma2(f32x2{o[1][2], o[1][3]}, r1, up2(hr.w)))};
  };
  auto put = [&](v4u r, int col) {
    const int ln = LN();
    char* p = out + (size_t)(blk * 32 + (ln & 15)) * ROWB + (col + 4 * (ln >> 4)) * 2;
    *(v2u*)p = v2u{r.x, r.y};
    *(v2u*)(p + 16 * ROWB) = v2u{r.z, r.w};
  };

  auto run = [&](auto GG) {
  constexpr int G = decltype(GG)::v;
  if constexpr (G == 1) __builtin_amdgcn_s_setprio(1);
  sfor<0, PGS[G].npre>([&](auto I) { constexpr Op o = PGS[G].pre[decltype(I)::v]; doop(IC<o.k>{}, IC<o.a>{}, IC<o.b>{}); });
  sfor<0, NS>([&](auto SS) {
    constexpr int st = decltype(SS)::v;
    auto ops_at = [&](auto PP) {
      constexpr int p = decltype(PP)::v;
      sfor<first_op(G, st, p), first_op(G, st, p + 1)>([&](auto I) { constexpr Op o = PGS[G].ops[st][decltype(I)::v]; doop(IC<o.k>{}, IC<o.a>{}, IC<o.b>{}); });
    };
    auto prep = [&](auto UU, auto NOP) {
      constexpr int U = decltype(UU)::v, k = u_k(U);
      if constexpr (kfirst(U) && (st < NS1 || a2_loaded(k))) {
        v4u& x = st < NS1 ? A1r[k][0] : A2r[k][0];
        v4u& y = st < NS1 ? A1r[k][1] : A2r[k][1];
        asm volatile("s_waitcnt vmcnt(%2)" : "+v"(x), "+v"(y) : "i"(PGS[G].awt[U]) : "memory");
        if constexpr (st < NS1 || k == 7 || k == 8) {
          unsigned a0 = x.z, a1 = x.w, b0 = y.x, b1 = y.y;
          if constexpr (decltype(NOP)::v) asm volatile("v_swap_b32 %0, %2\n\tv_swap_b32 %1, %3\n\ts_nop 1" : "+v"(a0), "+v"(a1), "+v"(b0), "+v"(b1));
          else asm volatile("v_swap_b32 %0, %2\n\tv_swap_b32 %1, %3" : "+v"(a0), "+v"(a1), "+v"(b0), "+v"(b1));
          x.z = a0; x.w = a1; y.x = b0; y.y = b1;
        }
      }
    };
    if constexpr (st == S_V) { pin(fl); if (!__all(fl == tag)) wait_flags<14>(srd, OFF_FV + w * 14 * 128, tag, LN()); }
    asm volatile("s_waitcnt vmcnt(%0)\n\ts_barrier" ::"i"(PGS[G].wait[st]) : "memory");
    if constexpr (st == S_V && G == 0) { if (blockIdx.x == 0 && w == 7) set_flag(srd, OFF_EP, tag, LN()); }
    sfor<0, 4>([&](auto CC) {
      constexpr int cl = decltype(CC)::v;
      if constexpr (st == s_pub(cl) && G == 0) { if ((w & 3) == 0) set_flag(srd, cnt + cl * 32768 + nt * 128, tag, LN()); }
      if constexpr (st == s_chk(cl)) {
        pin(pollv[cl]);
        if (!__all(pollv[cl] == tag)) wait_flags<4>(srd, cnt + cl * 32768, tag, LN());
      }
    });
    if constexpr (st == S_FA && G == 0) { if ((w & 3) == 0) set_flag(srd, wf + s8 * 128, tag, LN()); }
    if constexpr (st == S_FM && G == 0) { if ((w & 3) == 0) set_flag(srd, wf + 32768 + s8 * 128, tag, LN()); }
    if constexpr (st == S_FB && G == 0) { if ((w & 3) == 1) set_flag(srd, wf + 65536 + s8 * 128, tag, LN()); }
    if constexpr (st == S_QB) { pin(flx[2]); early_ok = early_ok && __all(flx[2] == tag); }
    if constexpr (st == S_QA) { pin(flx[0]); early_ok = early_ok && __all(flx[0] == tag); }
    if constexpr (st == S_CM) { pin(flx[1]); if (!__all(flx[1] == tag)) wait_flags<8>(srd, wf + 32768, tag, LN()); }
    if constexpr (st == 0) sfor<0, LA>(rdB);
    ops_at(IC<0>{});
    __builtin_amdgcn_sched_barrier(0);
    sfor<0, 9>([&](auto TT) {
      constexpr int i = decltype(TT)::v, U = 9 * st + i, k = u_k(U), t = u_t(U);
      if constexpr (i == 0) { prep(IC<U + 1>{}, IC<0>{}); prep(IC<U>{}, IC<1>{}); }
      if constexpr (U == 18 * WA - 1) {
#pragma unroll
        for (int m = 0; m < 2; ++m) { mean[m] = sum[m][0] * (1.0f / D); unsigned nm = pk(-mean[m], -mean[m]); A1r[18][m] = v4u{nm, nm, nm, nm}; }
      }
      if constexpr (st < NS1) {
        acc[t][0] = mfma(Bq[U % (LA + 1)], A1r[k][0], acc[t][0]);
        pin(acc[t][0]);
        rdB(IC<U + LA>{});
        __builtin_amdgcn_sched_barrier(0);
        acc[t][1] = mfma(Bq[U % (LA + 1)], A1r[k][1], acc[t][1]);
        pin(acc[t][1]);
        if constexpr (i < 7) prep(IC<U + 2>{}, IC<0>{});
        if constexpr (U < 18 * WA && t == 0) { sum[0] = mfma(ones, A1r[k][0], sum[0]); sum[1] = mfma(ones, A1r[k][1], sum[1]); pin(sum[0]); pin(sum[1]); }
        if constexpr (U < 18 * WA && t == NA) {
          gram[0] = mfma(A1r[k][0], A1r[k][0], gram[0]); gram[1] = mfma(A1r[k][1], A1r[k][1], gram[1]);
          pin(gram[0]); pin(gram[1]);
        }
      } else {
        acc2[t][0] = mfma(Bq[U % (LA + 1)], A2r[k][0], acc2[t][0]);
        pin(acc2[t][0]);
        rdB(IC<U + LA>{});
        __builtin_amdgcn_sched_barrier(0);
        acc2[t][1] = mfma(Bq[U % (LA + 1)], A2r[k][1], acc2[t][1]);
        pin(acc2[t][1]);
        if constexpr (i < 7) prep(IC<U + 2>{}, IC<0>{});
      }
      ops_at(IC<i + 1>{});
      __builtin_amdgcn_sched_barrier(0);
    });
  });
  static_assert(TA == 4, "slots 5..7 carry markers");
  sfor<TA + 1, 8>([&](auto I) { if constexpr (s_ql(decltype(I)::v) >= NS) doop(IC<QL>{}, I, IC<0>{}); });
  asm volatile("s_waitcnt vmcnt(%2)" : "+v"(hres[0]), "+v"(hres[1]) : "i"(PGS[G].qw[0] < PGS[G].qw[8] ? PGS[G].qw[0] : PGS[G].qw[8]) : "memory");
  if (!early_ok) {
    asm volatile("s_waitcnt vmcnt(0)" : "+v"(q[1]), "+v"(q[2]), "+v"(q[3]), "+v"(q[4]), "+v"(q[5])::"memory");
    wait_flags<8>(srd, wf, tag, LN());
    doop(IC<QL>{}, IC<0>{}, IC<0>{});
    reload5(q[1], q[2], q[3], q[4], q[5], srd, l16, inb + s8 * INB + w * 1024 - 2 * 8192);
  }
  v4u r9 = hres[1];
  if (w == s8) {
#pragma unroll
    for (int i = 0; i < 7; ++i) addq(acc2[0], *(const v4u*)(sm + L_Q9 + i * 1024 + l16));
    r9 = fin(acc2[0], hres[1]);
  }
  sfor<1, 5>([&](auto I) {
    constexpr int i = decltype(I)::v;
    waitv<PGS[G].qw[i]>(q[i]);
    addq(acc2[8], q[i]);
    pin(acc2[8][0]); pin(acc2[8][1]);
  });
  asm volatile("s_waitcnt vmcnt(0)" : "+v"(q[5]), "+v"(q[6]), "+v"(q[7])::"memory");
#pragma nounroll
  for (int spin = 0; spin < (1 << 17); ++spin) {
    const v4u m = __builtin_elementwise_max(__builtin_elementwise_max(q[5], q[6]), q[7]);
    const unsigned m0 = m.x > m.y ? m.x : m.y, m1 = m.z > m.w ? m.z : m.w;
    if (__all((m0 > m1 ? m0 : m1) != ~0u)) break;
    reload3(q[5], q[6], q[7], srd, l16, inb + s8 * INB + w * 1024);
  }
  addq(acc2[8], q[5]); addq(acc2[8], q[6]); addq(acc2[8], q[7]);
  put(fin(acc2[8], hres[0]), 144 * nt + 16 * s8);
  if (w == s8) put(r9, 144 * nt + 128);
  };
  if (w >> 2) run(IC<0>{}); else run(IC<1>{});
}

extern "C" __global__ void mega_end() {}

extern "C" void kernel_launch(void* const* d_in, const int* in_sizes, int n_in, void* d_out, int out_size, void* d_ws, size_t ws_size,
                              hipStream_t stream) {
  (void)hipFuncSetAttribute((const void*)mega, hipFuncAttributeMaxDynamicSharedMemorySize, L_ALL);
  mega<<<256, 512, L_ALL, stream>>>((const char*)d_in[0], (const char*)d_in[1], (const char*)d_in[2], (const char*)d_in[3], (const char*)d_in[4],
                                    (const unsigned*)d_in[5], (const char*)d_in[7], (const char*)d_in[8], (char*)d_ws, (char*)d_out);
}
```

```cpp
#include <hip/hip_runtime.h>
#include <hip/hip_bf16.h>
#include <stdint.h>

typedef unsigned v4u __attribute__((ext_vector_type(4)));
typedef unsigned v2u __attribute__((ext_vector_type(2)));
typedef float f32x4 __attribute__((ext_vector_type(4)));
typedef float f32x2 __attribute__((ext_vector_type(2)));
typedef __bf16 bf16x2 __attribute__((ext_vector_type(2)));
typedef __bf16 bf16x8 __attribute__((ext_vector_type(8)));
typedef short s16x4 __attribute__((ext_vector_type(4)));
typedef short s16x2 __attribute__((ext_vector_type(2)));
#define LAS __attribute__((address_space(3)))
#define DEVI __device__ __forceinline__

static constexpr int S = 2048, D = 576, F = 4608;
static constexpr int ROWB = D * 2;

static constexpr int OFF_EP = 0;
static constexpr int OFF_F0 = 1024;
static constexpr int OFF_FW = OFF_F0 + 144 * 128;
static constexpr int OFF_FV = OFF_FW + 112 * 128;
static constexpr int OFF_FH = OFF_FV + 112 * 128;
static constexpr int OFF_FC = OFF_FH + 1152 * 128;
static constexpr int OFF_FP = OFF_FC + 1024 * 128;
static constexpr int OFF_ST1 = OFF_FP + 2048 * 128;
static constexpr int OFF_KV = OFF_ST1 + 16384;
static constexpr int OFF_XT = OFF_KV + S * D * 2;
static constexpr int OFF_H = OFF_XT + S * D * 2;
static constexpr int OFF_W1 = OFF_H + S * D * 2;
static constexpr int OFF_W2 = OFF_W1 + 32 * 172 * 1024;
static constexpr int OFF_HID = OFF_W2 + D * F * 2;
static constexpr int OFF_IN = OFF_HID + S * F * 2;
static constexpr int INB = 27 * 1024;
static constexpr int OFF_ST = OFF_IN + 256 * INB;

DEVI float lo2f(unsigned u) { return __uint_as_float(u << 16); }
DEVI float hi2f(unsigned u) { return __uint_as_float(u & 0xffff0000u); }
DEVI f32x2 up2(unsigned u) { return f32x2{lo2f(u), hi2f(u)}; }
DEVI unsigned pk2(f32x2 v) { return __builtin_bit_cast(unsigned, __builtin_convertvector(v, bf16x2)); }
DEVI unsigned pk(float a, float b) { return pk2(f32x2{a, b}); }
DEVI f32x2 fma2(f32x2 a, f32x2 b, f32x2 c) { return __builtin_elementwise_fma(a, b, c); }
DEVI float sum2(unsigned u, float c) { const bf16x2 one = {(__bf16)1.0f, (__bf16)1.0f}; return __builtin_amdgcn_fdot2_f32_bf16(__builtin_bit_cast(bf16x2, u), one, c, false); }
DEVI float rbf(float a) { return lo2f(pk(a, a)); }
DEVI unsigned relu2(unsigned u) { s16x2 z = {0, 0}; return __builtin_bit_cast(unsigned, __builtin_elementwise_max(__builtin_bit_cast(s16x2, u), z)); }
DEVI f32x4 mfma(v4u a, v4u b, f32x4 c) {
  return __builtin_amdgcn_mfma_f32_16x16x32_bf16(__builtin_bit_cast(bf16x8, a), __builtin_bit_cast(bf16x8, b), c, 0, 0, 0);
}
template <int N> struct IC { static constexpr int v = N; };
template <int I, int N, class Fn> DEVI void sfor(Fn&& f) { if constexpr (I < N) { f(IC<I>{}); sfor<I + 1, N>(f); } }
template <int CTRL> DEVI float dpp_add(float v) {
  return v + __builtin_bit_cast(float, __builtin_amdgcn_update_dpp(0, __builtin_bit_cast(int, v), CTRL, 0xf, 0xf, true));
}
DEVI float row16_sum(float v) { v = dpp_add<0xB1>(v); v = dpp_add<0x4E>(v); v = dpp_add<0x141>(v); return dpp_add<0x140>(v); }
DEVI v4u mksrd(const void* p, unsigned bytes) { return v4u{(unsigned)(size_t)p, (unsigned)((size_t)p >> 32) & 0xffffu, bytes, 0x00020000u}; }

#define BUFLD(name, T, text)                                                                                     \
  template <int IMM, class U> DEVI void name(T& d, v4u srd, unsigned voff, U&& soff) {                           \
    soff = __builtin_amdgcn_readfirstlane(soff);                                                                 \
    asm volatile("" : "+s"(soff));                                                                               \
    asm volatile(text : "=v"(d) : "v"(voff), "s"(srd), "s"(soff + IMM) : "memory");                              \
  }
#define BUFST(name, T, text)                                                                                     \
  template <int IMM, class U> DEVI void name(T d, v4u srd, unsigned voff, U&& soff) {                            \
    soff = __builtin_amdgcn_readfirstlane(soff);                                                                 \
    asm volatile("" : "+s"(soff));                                                                               \
    asm volatile(text ::"v"(d), "v"(voff), "s"(srd), "s"(soff + IMM) : "memory");                                \
  }
BUFLD(bl16, v4u, "buffer_load_dwordx4 %0, %1, %2, %3 offen")
BUFLD(bl8, v2u, "buffer_load_dwordx2 %0, %1, %2, %3 offen")
BUFLD(bl16c, v4u, "buffer_load_dwordx4 %0, %1, %2, %3 offen sc1")
BUFLD(bl8c, v2u, "buffer_load_dwordx2 %0, %1, %2, %3 offen sc1")
BUFLD(bl4c, unsigned, "buffer_load_dword %0, %1, %2, %3 offen sc1")
BUFST(bs16c, v4u, "buffer_store_dwordx4 %0, %1, %2, %3 offen sc1\n\ts_nop 1")
BUFST(bs8c, v2u, "buffer_store_dwordx2 %0, %1, %2, %3 offen sc1")
BUFST(bs4c, unsigned, "buffer_store_dword %0, %1, %2, %3 offen sc1")
template <int IMM, int LIMM, class U, class V> DEVI void blds16(v4u srd, unsigned voff, U&& soff, V&& lds) {
  asm volatile("" : "+s"(soff), "+s"(lds));
  asm volatile("s_mov_b32 m0, %3\n\ts_nop 0\n\tbuffer_load_dwordx4 %0, %1, %2 offen lds" ::"v"(voff), "s"(srd), "s"(soff + IMM), "s"(lds + LIMM) : "memory");
}
template <int IMM, int LIMM, class U, class V> DEVI void blds16c(v4u srd, unsigned voff, U&& soff, V&& lds) {
  asm volatile("" : "+s"(soff), "+s"(lds));
  asm volatile("s_mov_b32 m0, %3\n\ts_nop 0\n\tbuffer_load_dwordx4 %0, %1, %2 offen sc1 lds" ::"v"(voff), "s"(srd), "s"(soff + IMM), "s"(lds + LIMM) : "memory");
}
DEVI void blds4c(v4u srd, unsigned voff, unsigned soff, unsigned lds) {
  asm volatile("s_mov_b32 m0, %3\n\ts_nop 0\n\tbuffer_load_dword %0, %1, %2 offen sc1 lds" ::"v"(voff), "s"(srd), "s"(soff), "s"(lds) : "memory");
}
DEVI void blds4(v4u srd, unsigned voff, unsigned lds) {
  asm volatile("s_mov_b32 m0, %2\n\ts_nop 0\n\tbuffer_load_dword %0, %1, 0 offen lds" ::"v"(voff), "s"(srd), "s"(lds) : "memory");
}
template <class T> DEVI void pin(T& x) { asm volatile("" : "+v"(x)); }
DEVI void lds_rd64(v2u& d, unsigned addr, int off) { asm volatile("ds_read_b64 %0, %1 offset:%2" : "=v"(d) : "v"(addr), "i"(off) : "memory"); }
DEVI void lds_rd128(v4u& d, unsigned addr, int off) { asm volatile("ds_read_b128 %0, %1 offset:%2" : "=v"(d) : "v"(addr), "i"(off) : "memory"); }
DEVI void set_flag(v4u srd, unsigned off, unsigned tag, int lane) { bs4c<0>(tag, srd, lane < 32 ? lane * 4 : 0x80000000u, off); }
DEVI void wait_flags_v(v4u srd, unsigned off, unsigned vo, unsigned tag) {
#pragma nounroll
  for (int spin = 0; spin < (1 << 17); ++spin) {
    unsigned v;
    asm volatile("buffer_load_dword %0, %1, %2, %3 offen sc1\n\ts_waitcnt vmcnt(0)" : "=&v"(v) : "v"(vo), "s"(srd), "s"(off) : "memory");
    vo = v == tag ? 0x80000000u : vo;
    if (__all((int)vo < 0)) break;
    __builtin_amdgcn_s_sleep(2);
  }
}
template <int N> DEVI void wait_flags(v4u srd, unsigned off, unsigned tag, int lane) { wait_flags_v(srd, off, lane < N ? lane * 128 : 0x80000000u, tag); }
DEVI void wait_stats(v4u& v, v4u srd, unsigned vo, unsigned off, unsigned tag) {
#pragma nounroll
  for (int spin = 0; spin < (1 << 17) && !__all(v.y == tag && v.w == tag); ++spin)
    asm volatile("buffer_load_dwordx4 %0, %1, %2, %3 offen sc1\n\ts_waitcnt vmcnt(0)" : "=&v"(v) : "v"(vo), "s"(srd), "s"(off) : "memory");
}

namespace sch {
constexpr int UF = 126;
constexpr int KT = 10, KR = 18 - KT;
#define HD __host__ __device__ constexpr
HD int p1_unit(int kl, int tau) { return tau < 2 ? 3 * kl + tau : tau == 8 ? 3 * kl + 2 : 19 + 38 * (tau >> 1) + 2 * kl + (tau & 1); }
HD int p2_unit(int kk, int tp) {
  return tp >= 7 ? UF + 2 * kk + (tp - 7) : kk < KT ? 7 * kk + tp : tp < 3 ? 7 * KT + 3 * (kk - KT) + tp : 7 * KT + ((tp - 1) | 1) * KR + 2 * (kk - KT) + ((tp - 1) & 1);
}
HD int kpair(int p) { return p == 0 ? 0 : 4 + 8 * p; }
HD int kprime(int pr, int r) { return r == 4 ? 8 + pr : kpair(r % 5) + 2 * pr + r / 5; }

constexpr int ring_pos(int f) { return f < 304 ? f % 152 : (f - 304) % 120; }
constexpr int ring_prev(int f) { return f < 424 ? f - 152 : f - 120; }
constexpr int LA = 4, CAP = 5, WST = 3;
constexpr int N1 = 171, NU = N1 + 162, NS1 = 19, NS = 37, NR = 62;
struct UMap { int k[NU], t[NU], km[NU]; };
constexpr UMap mkmap() {
  UMap M{};
  for (int k = 0; k < 19; ++k) for (int t = 0; t < 9; ++t) { M.k[p1_unit(k, t)] = k; M.t[p1_unit(k, t)] = t; }
  for (int k = 0; k < 18; ++k) for (int t = 0; t < 9; ++t) { M.k[N1 + p2_unit(k, t)] = k; M.t[N1 + p2_unit(k, t)] = t; }
  for (int U = 0; U < NU; ++U) { int m = (U == 0 || U == N1) ? -1 : M.km[U - 1]; m = M.k[U] > m ? M.k[U] : m; M.km[U] = m > 17 ? 17 : m; }
  return M;
}
constexpr UMap UM = mkmap();
constexpr int u_k(int U) { return UM.k[U]; }
constexpr int u_t(int U) { return UM.t[U]; }
constexpr int c_sth(int i) { return i < 3 ? 0 : (i - 1) >> 1; }
constexpr int s_sth(int G, int i) {
  int st = 0;
  for (int k = 0; k <= i; ++k) { st += k == 8 ? 0 : 1; int rdy = (57 + 38 * c_sth(k) + 8) / 9 - G; if (st < rdy) st = rdy; }
  return st;
}
constexpr int s_pub(int c) { return s_sth(0, 2 + 2 * c) + 3; }
constexpr int s_chk(int c) { return s_pub(c) + (c == 3 ? 4 : 5); }
constexpr int xdone(int gr) { return N1 + 7 * KT + (3 + 2 * gr) * KR; }
constexpr int s_x(int gr) { return (xdone(gr) - 1) / 9 + 1; }
constexpr int xslot(int t) { return t < 4 ? t : t < 7 ? t - 4 : t == 7 ? 3 : 0; }
constexpr int S_FL = s_x(2) + 2;
constexpr int a2class(int i) { return i < 12 ? 0 : (i - 4) >> 3; }
constexpr int piece_lo(int U) { return U < N1 ? U : 172 + 2 * (U - N1); }
constexpr int piece_hi(int U) { return U < N1 ? U : 172 + 2 * (U - N1) + 1; }
constexpr int kmaxu(int U1) { return UM.km[U1]; }
constexpr int kmax(int st) { return kmaxu(9 * st + 8); }
constexpr bool kfirst(int U) { return u_k(U) < 18 && (U == 0 || U == N1 || kmaxu(U) > kmaxu(U - 1)); }
constexpr int need_round(int st) { int U = 9 * st + 8 + LA; return piece_hi(U > NU - 1 ? NU - 1 : U) >> 3; }
constexpr int gl_need(int r) { int st = 0; while (need_round(st) < r) ++st; return st; }
constexpr int a_need(int ph, int i) { int st = ph ? NS1 : 0; while (2 * kmax(st) + 1 < i) ++st; return st; }
enum { GL, A1, A2, STH, POLL, XR, STP, HRES, FLL, VAR, WAITH, POLLV, WAITW };
constexpr int S_V = 8;
constexpr int S_G0 = 7;
struct Op { int k, a, b, pos; };
struct Prog {
  Op pre[40]; int npre;
  Op ops[NS][24]; int nops[NS];
  int wait[NS], awt[NU];
  bool ok;
};
constexpr Prog build(int G) {
  Prog P{};
  P.ok = true;
  int cnt = 0, gl = -1, a1 = -1, a2 = -1;
  int glseq[NR] = {}, a1seq[36] = {}, a2seq[36] = {}, extra[NS + 8] = {};
  {
    int n = 0;
    auto pre = [&](int k, int a, int b, int wgt) { P.pre[n++] = Op{k, a, b, 0}; cnt += wgt; return cnt; };
    pre(WAITW, 0, 0, 0);
    while (gl < 8 - 1) { ++gl; glseq[gl] = pre(GL, gl, 0, 1); }
    pre(WAITH, 0, 0, 0);
    while (a1 < 2 * kmax(G ? 1 : 1 - 1) + 1) { ++a1; a1seq[a1] = pre(A1, a1 >> 1, a1 & 1, 1); }
    P.npre = n;
  }
  for (int st = 0; st < NS; ++st) {
    if (gl < need_round(st) || (st < NS1 ? a1 : a2) < 2 * kmax(st) + 1) P.ok = false;
    int need = glseq[need_round(st)];
    need = extra[st] > need ? extra[st] : need;
    P.wait[st] = cnt - need > 63 ? 63 : cnt - need;
    Op ld[24] = {}; int nl = 0, fixed = 0;
    for (int i = 0; i < 9; ++i) if (s_sth(G, i) == st) fixed += WST;
    for (int gr = 0; gr < 3; ++gr) if (st == s_x(gr)) fixed += (gr ? 1 : 2) * WST;
    {
      int g2 = gl, b1 = a1, b2 = a2;
      const int dead = 9 * (st + G) - 133;
      for (int budget = CAP - fixed;; --budget) {
        const int done = st == 0 ? -1 : piece_hi(9 * st - 1);
        const int BIG = 1000;
        int ng = (g2 < NR - 1 && ring_prev(8 * (g2 + 1) + 7) <= done && (g2 + 1 < 21 || st >= S_V)) ? gl_need(g2 + 1) : BIG;
        int na = BIG;
        if (b1 < 35) na = a_need(0, b1 + 1);
        else if (b2 < 35 && st >= s_chk(a2class(b2 + 1)) && b2 + 1 < (dead > 0 ? dead : 0) + 8) na = a_need(1, b2 + 1);
        int m = ng < na ? ng : na;
        if (m == BIG || (budget <= 0 && m - st > (b1 < 35 ? 1 : 2) + G)) break;
        if (na <= ng) { if (b1 < 35) { ++b1; ld[nl++] = Op{A1, b1 >> 1, b1 & 1, 0}; } else { ++b2; ld[nl++] = Op{A2, b2 >> 1, b2 & 1, 0}; } }
        else { ++g2; ld[nl++] = Op{GL, g2, 0, 0}; }
      }
    }
    int n = 0, cmid = 0;
    auto req = [&](int at, int seq) { if (extra[at] < seq) extra[at] = seq; };
    auto opp = [&](int pos, int k, int a, int b, int wgt) { P.ops[st][n++] = Op{k, a, b, pos}; cnt += wgt; return cnt; };
    auto op = [&](int k, int a, int b, int wgt) { return opp(G ? 9 : 0, k, a, b, wgt); };
    auto stp = [&]() { for (int gr = 0; gr < 3; ++gr) if (st == s_x(gr)) { op(STP, gr, 0, 1); if (gr == 0) op(STP, 3, 0, 0); } };
    for (int gr = 0; gr < 3; ++gr) if (st == s_x(gr)) { opp(0, XR, 1 + 2 * gr, 0, 0); opp(0, XR, 2 + 2 * gr, 0, 0); if (gr == 0) opp(0, XR, 0, 0, 0); }
    for (int c = 0; c < 4; ++c) if (st == s_chk(c) - 2) req(s_chk(c), opp(0, POLL, c, 0, 1));
    if (st == S_V - 3) req(S_V - 1, opp(0, POLLV, 0, 0, 1));
    if (st == NS - 2) req(NS - 1, opp(0, FLL, 0, 0, 1));
    if (G) { cmid = cnt; stp(); }
    for (int il = 0; il < nl; ++il) {
      int q = op(ld[il].k, ld[il].a, ld[il].b, 1);
      if (ld[il].k == GL) glseq[++gl] = q; else if (ld[il].k == A1) a1seq[++a1] = q; else a2seq[++a2] = q;
    }
    if (!G) stp();
    for (int h = 0; h < 9; ++h) if (s_sth(G, h) == st) req(s_pub(c_sth(h)), op(STH, h, 0, 1));
    if (st == s_x(2) - 2) { op(HRES, 0, 0, 1); op(HRES, 1, 0, 1); op(HRES, 2, 0, 1); }
    if (st == 13 - G) op(VAR, 0, 0, 0);
    if (!G) cmid = cnt;
    for (int U = 9 * st; U < 9 * st + 9; ++U) if (kfirst(U)) { int d = cmid - (st < NS1 ? a1seq : a2seq)[2 * u_k(U) + 1]; P.awt[U] = d > 63 ? 63 : d; }
    P.nops[st] = n;
    if (n > 24) P.ok = false;
  }
  return P;
}
constexpr Prog PGS[2] = {build(0), build(1)};
static_assert(PGS[0].ok && PGS[1].ok, "schedule: an operand is needed before it is requested");
static_assert(S_FL < NS - 1 && s_x(0) < s_x(1) - 1 && s_x(1) < s_x(2), "exchange steps");
constexpr int first_op(int G, int st, int i) { int n = 0; while (n < PGS[G].nops[st] && PGS[G].ops[st][n].pos < i) ++n; return n; }
constexpr bool xbar(int st) { return st == s_x(0) || st == s_x(1) || st == s_x(2); }
}

static_assert(sch::piece_lo(9 * ((sch::xdone(0) - 3) / 9)) > 303, "exchange area still holds ring pieces");
static constexpr int L_X = 120 * 1024;
static constexpr int L_RS = L_X + 32768;
static constexpr int L_FFN = L_RS + 512, L_ALL = L_FFN + 256;

static constexpr int KF_CP = 4160, KF_CH = 4 * KF_CP;
static constexpr int L_KF = 0, L_KV = 4 * KF_CH, L_RED = L_KV + 18 * 1024, L_Z = L_RED;
static constexpr int L_XS = 98304, L_XI = L_XS + 256;
static_assert(L_XI + 2 * 20736 <= L_FFN, "LDS");
static constexpr int L_STG = L_RED, STGP = 1088, STGW = 5 * STGP;
static_assert(L_STG + 8 * STGW <= L_FFN && L_Z + 8 * KF_CP <= L_FFN && 8 * 12288 + 256 <= L_FFN, "LDS");

extern "C" __global__ __launch_bounds__(512, 2) void mega(const char* __restrict__ xg, const char* __restrict__ kg, const char* __restrict__ vg,
                                                       const char* __restrict__ skg, const char* __restrict__ kfg, const unsigned* __restrict__ ssmD,
                                                       const char* __restrict__ w1g, const char* __restrict__ w2g, char* __restrict__ ws,
                                                       char* __restrict__ out) {
  using namespace sch;
  extern __shared__ __attribute__((aligned(16))) char sm[];
  asm volatile("" ::"s"(xg), "s"(kg), "s"(vg), "s"(skg), "s"(kfg), "s"(ssmD), "s"(w1g), "s"(w2g), "s"(ws), "s"(out));
  const int tid = threadIdx.x, lane = tid & 63;
  const int w = __builtin_amdgcn_readfirstlane(tid >> 6);
  const unsigned lbase = (unsigned)(size_t)(LAS char*)sm;
  const int g = lane >> 4, c = lane & 15;
  const unsigned l16 = lane * 16;
  const v4u srd = mksrd(ws, OFF_ST);
  unsigned tag, fwv;
  const int bx = blockIdx.x & 7, bi = blockIdx.x >> 3;
  const int mt8 = (bx >> 2) * 4 + (bi >> 3), j = 8 * (bx & 3) + (bi & 7);
  const unsigned fwo = (((min(lane, 11) >> 1) * 144 + ((9 * j) >> 1) + (min(lane, 11) & 1) * 4) >> 3) * 128;

  if (__builtin_expect(blockIdx.x < 144, 1)) {
    const int cg = 18 * (blockIdx.x & 7) + (blockIdx.x >> 3), sg = cg >> 4, rb = cg & 15, ch = w & 3, eh = w >> 2;
    unsigned epv;
    unsigned stg = lbase + L_STG + w * STGW;
    const char* stgp = sm + L_STG + w * STGW;
    {
      const unsigned vo = (128 * rb + 16 * w - 8 + (lane >> 3)) * ROWB + sg * 128 + (lane & 7) * 16;
      const v4u sk_ = mksrd(kg, S * ROWB), sv_ = mksrd(vg, S * ROWB);
      blds16<0, STGP>(sk_, vo + 8 * ROWB, 0u, stg);
      blds16<0, 2 * STGP>(sk_, vo + 16 * ROWB, 0u, stg);
      blds16<0, 0>(sk_, vo, 0u, stg);
      blds16<0, 3 * STGP>(sv_, vo + 8 * ROWB, 0u, stg);
      blds16<0, 4 * STGP>(sv_, vo + 16 * ROWB, 0u, stg);
    }
    if (w == 7 && blockIdx.x < 96) {
      unsigned lo, hi, len;
      asm volatile("s_getpc_b64 vcc\n\ts_add_u32 %0, vcc_lo, mega@rel32@lo+4\n\ts_addc_u32 %1, vcc_hi, mega@rel32@hi+12\n\ts_mov_b32 %2, mega_end-mega"
                   : "=s"(lo), "=s"(hi), "=s"(len)::"scc", "vcc");
      blds4(v4u{lo, hi & 0xffffu, len > 0x40000u ? 0u : len, 0x00020000u}, ((blockIdx.x >> 3) * 64 + lane) * 128, lbase + L_FFN);
    }
    bl4c<OFF_EP>(epv, srd, 0u, 0u);
    const int cgl = lane >> 2, tcl = (lane >> 1) & 1, cpl = lane & 1;
    v4u skw, kw[2];
    bl16<0>(skw, mksrd(skg, D * 8), (64 * sg + 4 * cgl + 2 * cpl) * 8, 0u);
    const v2u kmid = *(const v2u*)(kfg + (size_t)(4 * cg + ch) * 4096 + 2048);
    const unsigned dpair = ssmD[2 * cg + (ch >> 1)];
    if (tid < 128) *(v4u*)(sm + L_KV + tid * 16) = v4u{0u, 0u, 0u, 0u};
    asm volatile("s_waitcnt vmcnt(0)" : "+v"(skw), "+v"(epv)::"memory");
    const v4u skf = mksrd(kfg, S * D * 2);
    bl16<0>(kw[0], skf, l16, (unsigned)((4 * cg + ch) * 4096 + eh * 2048));
    bl16<1024>(kw[1], skf, l16, (unsigned)((4 * cg + ch) * 4096 + eh * 2048));
    tag = __builtin_amdgcn_readfirstlane(epv) + 1u;
    {
      const char* p = stgp + tcl * STGP + (2 * cgl + cpl) * 4;
      f32x2 kf[11], r[8];
#pragma unroll
      for (int i = 0; i < 11; ++i) kf[i] = up2(*(const unsigned*)(p + ((5 + i) >> 3) * STGP + ((5 + i) & 7) * 128));
      const f32x2 s0 = {lo2f(skw.x), lo2f(skw.z)}, s1 = {hi2f(skw.x), hi2f(skw.z)}, s2 = {lo2f(skw.y), lo2f(skw.w)}, s3 = {hi2f(skw.y), hi2f(skw.w)};
#pragma unroll
      for (int i = 0; i < 8; ++i)
        r[i] = fma2(kf[i + 3], s0, fma2(kf[i + 2], s1, fma2(kf[i + 1], s2, kf[i] * s3))) * up2(*(const unsigned*)(p + 3 * STGP + i * 128));
      unsigned so = OFF_KV + sg * 262144 + (8 * rb + w) * 128;
      unsigned o0[4] = {pk(r[7].x, r[6].x), pk(r[5].x, r[4].x), pk(r[3].x, r[2].x), pk(r[1].x, r[0].x)};
      unsigned o1[4] = {pk(r[7].y, r[6].y), pk(r[5].y, r[4].y), pk(r[3].y, r[2].y), pk(r[1].y, r[0].y)};
#pragma unroll
      for (int i = 0; i < 4; ++i) {
        const int a = o0[i], b = o1[i];
        o0[i] = __builtin_amdgcn_update_dpp(a, b, 0x114, 0xf, 0xa, false);
        o1[i] = __builtin_amdgcn_update_dpp(b, a, 0x104, 0xf, 0x5, false);
      }
      bs16c<0>(v4u{o0[0], o0[1], o0[2], o0[3]}, srd, (lane >> 3) * 32768 + (lane & 7) * 16, so);
      bs16c<16384>(v4u{o1[0], o1[1], o1[2], o1[3]}, srd, (lane >> 3) * 32768 + (lane & 7) * 16, so);
    }
    asm volatile("s_waitcnt vmcnt(2)" : "+v"(kw[0]), "+v"(kw[1])::"memory");
    {
      auto put = [&](v4u a, v2u n, int X) {
        unsigned a01 = __builtin_amdgcn_alignbit(a.y, a.x, 16), a12 = __builtin_amdgcn_alignbit(a.z, a.y, 16), a23 = __builtin_amdgcn_alignbit(a.w, a.z, 16);
        unsigned a3n = __builtin_amdgcn_alignbit(n.x, a.w, 16), ann = __builtin_amdgcn_alignbit(n.y, n.x, 16);
        char* p = sm + L_KF + ch * KF_CH + X * 16;
        *(v4u*)(p) = a;
        *(v4u*)(p + KF_CP) = v4u{a01, a12, a23, a3n};
        *(v4u*)(p + 2 * KF_CP) = v4u{a.y, a.z, a.w, n.x};
        *(v4u*)(p + 3 * KF_CP) = v4u{a12, a23, a3n, ann};
      };
      unsigned f0 = __builtin_amdgcn_readlane(kw[1].x, 0), f1 = __builtin_amdgcn_readlane(kw[1].y, 0);
      v2u n0 = {(unsigned)__builtin_amdgcn_update_dpp((int)f0, (int)kw[0].x, 0x130, 0xf, 0xf, false),
                (unsigned)__builtin_amdgcn_update_dpp((int)f1, (int)kw[0].y, 0x130, 0xf, 0xf, false)};
      put(kw[0], n0, 128 * eh + lane + 2);
      asm volatile("s_waitcnt vmcnt(0)\n\ts_barrier" ::: "memory");
      if (w == 0) set_flag(srd, OFF_F0 + cg * 128, tag, lane);
      v2u n1 = {(unsigned)__builtin_amdgcn_update_dpp((int)kmid.x, (int)kw[1].x, 0x130, 0xf, 0xf, false),
                (unsigned)__builtin_amdgcn_update_dpp((int)kmid.y, (int)kw[1].y, 0x130, 0xf, 0xf, false)};
      put(kw[1], n1, 128 * eh + 64 + lane + 2);
      if (eh == 0) {
        v2u k0 = {(unsigned)__builtin_amdgcn_readlane(kw[0].x, 0), (unsigned)__builtin_amdgcn_readlane(kw[0].y, 0)};
        if (lane < 2) put(v4u{0u, 0u, 0u, 0u}, lane == 1 ? k0 : v2u{0u, 0u}, lane);
      }
    }
    asm volatile("s_waitcnt lgkmcnt(0)\n\ts_barrier" ::: "memory");
    const unsigned abase = lbase + L_KF + ch * KF_CH + ((1 + c) & 3) * KF_CP + (4 * (g >> 1) - 2 * (g & 1) + 2 + ((1 + c) >> 2)) * 8 + eh * 256;
    v2u Al[2][8], Ah[2][8];
    v4u B[2][8];
    unsigned baddr[4];
#pragma unroll
    for (int e = 0; e < 4; ++e) { int x0 = 32 + 2 * c - 2 * (g >> 1) + (g & 1) - 4 * (4 * eh + e); baddr[e] = lbase + L_KV + x0 * 64 + ((ch + (x0 >> 2)) & 3) * 16; }
    auto rdA = [&](auto E, auto I) { constexpr int e = decltype(E)::v, i = decltype(I)::v; lds_rd64(Al[e & 1][i], abase, 64 * e + 512 * i); lds_rd64(Ah[e & 1][i], abase, 64 * e + 512 * i + 8); };
    auto rdB = [&](auto E, auto I) { constexpr int e = decltype(E)::v, i = decltype(I)::v; lds_rd128(B[e & 1][i], baddr[e], 2048 * i); };
    sfor<0, 8>([&](auto I) { rdA(IC<0>{}, I); });
    v4u AD;
    {
      const int jj = 7 + 8 * (g & 1) - c;
      const unsigned dv = (eh == 0 && g < 2 && jj >= 0 && jj < 8) ? ((ch & 1) ? dpair >> 16 : dpair & 0xffffu) << (16 * (jj & 1)) : 0u;
      AD = v4u{(jj >> 1) == 0 ? dv : 0u, (jj >> 1) == 1 ? dv : 0u, (jj >> 1) == 2 ? dv : 0u, (jj >> 1) == 3 ? dv : 0u};
      pin(AD);
    }
    wait_flags<2>(srd, OFF_F0 + (sg * 16 + 2 * w) * 128, tag, lane);
    {
      v4u kv[2];
      unsigned so = OFF_KV + cg * 16384 + w * 2048;
      bl16c<0>(kv[0], srd, l16, so);
      bl16c<1024>(kv[1], srd, l16, so);
      const int tc = 32 * w + 2 * (lane >> 3) + ((lane >> 1) & 1), cc = 2 * (lane & 1) + ((lane >> 2) & 1);
      char* p = sm + L_KV + (tc + 32) * 64 + ((cc + (tc >> 2)) & 3) * 16;
      asm volatile("s_waitcnt vmcnt(0)" : "+v"(kv[0]), "+v"(kv[1])::"memory");
      *(v4u*)p = kv[0];
      *(v4u*)(p + 1024) = kv[1];
    }
    asm volatile("s_waitcnt lgkmcnt(0)\n\ts_barrier"
                 : "+v"(Al[0][0]), "+v"(Al[0][1]), "+v"(Al[0][2]), "+v"(Al[0][3]), "+v"(Al[0][4]), "+v"(Al[0][5]), "+v"(Al[0][6]), "+v"(Al[0][7]),
                   "+v"(Ah[0][0]), "+v"(Ah[0][1]), "+v"(Ah[0][2]), "+v"(Ah[0][3]), "+v"(Ah[0][4]), "+v"(Ah[0][5]), "+v"(Ah[0][6]), "+v"(Ah[0][7])::"memory");
    sfor<0, 8>([&](auto I) { rdB(IC<0>{}, I); });
    f32x4 acc[8];
#pragma unroll
    for (int i = 0; i < 8; ++i) acc[i] = f32x4{0.f, 0.f, 0.f, 0.f};
    v4u xr[2], st1[2];
    f32x2 xf[4][2], qf[4][2];
    auto frA = [&](int s, int a) { return v4u{Al[s][a].x, Al[s][a].y, Ah[s][a].x, Ah[s][a].y}; };
    sfor<0, 4>([&](auto E) {
      constexpr int e = decltype(E)::v, s = e & 1;
      if constexpr (e > 0) {
        asm volatile("s_waitcnt lgkmcnt(0)"
                     : "+v"(Al[s][0]), "+v"(Al[s][1]), "+v"(Al[s][2]), "+v"(Al[s][3]), "+v"(Al[s][4]), "+v"(Al[s][5]), "+v"(Al[s][6]), "+v"(Al[s][7]),
                       "+v"(Ah[s][0]), "+v"(Ah[s][1]), "+v"(Ah[s][2]), "+v"(Ah[s][3]), "+v"(Ah[s][4]), "+v"(Ah[s][5]), "+v"(Ah[s][6]), "+v"(Ah[s][7]),
                       "+v"(B[s][0]), "+v"(B[s][1]), "+v"(B[s][2]), "+v"(B[s][3]), "+v"(B[s][4]), "+v"(B[s][5]), "+v"(B[s][6]), "+v"(B[s][7])::"memory");
      }
      __builtin_amdgcn_sched_barrier(0);
      if constexpr (e < 3) {
        sfor<0, 8>([&](auto BI) {
          constexpr int b = decltype(BI)::v;
          if constexpr (e == 0 && b < 2) asm volatile("s_waitcnt lgkmcnt(%1)" : "+v"(B[s][b]) : "i"(7 - b) : "memory");
          if constexpr (e == 0 && b == 2) asm volatile("s_waitcnt lgkmcnt(0)" : "+v"(B[s][2]), "+v"(B[s][3]), "+v"(B[s][4]), "+v"(B[s][5]), "+v"(B[s][6]), "+v"(B[s][7])::"memory");
          sfor<0, 8 - b>([&](auto AI) {
            constexpr int a = decltype(AI)::v, b0 = e == 0 ? 2 : 0, bb = b - b0;
            acc[a + b] = mfma(frA(s, a), B[s][b], acc[a + b]);
            pin(acc[a + b]);
            constexpr int per = e == 0 ? 3 : 2;
            if constexpr (bb >= 0 && a < 2 * per && per * bb + (a >> 1) < 8 && !(e == 0 && b == 3 && a == 5)) {
              if constexpr (a & 1) rdB(IC<e + 1>{}, IC<per * bb + (a >> 1)>{});
              else rdA(IC<e + 1>{}, IC<per * bb + (a >> 1)>{});
            }
          });
          if constexpr (e == 0) { acc[b] = mfma(AD, B[s][b], acc[b]); pin(acc[b]); if constexpr (b == 3) rdB(IC<1>{}, IC<5>{}); }
          if constexpr (e == 2 - 1 && b == 4) {
            bl16c<OFF_ST1>(st1[0], srd, l16, (unsigned)(w * 2048));
            bl16c<OFF_ST1 + 1024>(st1[1], srd, l16, (unsigned)(w * 2048));
          }
          if constexpr (e == 2 && b == 4) {
            asm volatile("s_waitcnt vmcnt(0)" : "+v"(st1[0]), "+v"(st1[1])::"memory");
            wait_stats(st1[0], srd, l16, OFF_ST1 + w * 2048, tag);
            wait_stats(st1[1], srd, l16, OFF_ST1 + w * 2048 + 1024, tag);
            unsigned xs = OFF_XT + cg * 16384 + w * 2048;
            bl16c<0>(xr[0], srd, l16, xs);
            bl16c<1024>(xr[1], srd, l16, xs);
          }
          __builtin_amdgcn_sched_barrier(0);
        });
      } else {
        sfor<0, 9>([&](auto CI) {
          constexpr int ct = decltype(CI)::v;
          if constexpr (ct < 8) sfor<0, ct + 1>([&](auto AI) { constexpr int a = decltype(AI)::v; acc[ct] = mfma(frA(s, a), B[s][ct - a], acc[ct]); pin(acc[ct]); });
          if constexpr (ct > 0) *(v2u*)(sm + L_Z + w * KF_CP + (256 * (ct - 1) + 16 * c + 4 * g) * 2) = v2u{pk(acc[ct - 1][0], acc[ct - 1][1]), pk(acc[ct - 1][2], acc[ct - 1][3])};
          __builtin_amdgcn_sched_barrier(0);
        });
      }
    });
    asm volatile("s_waitcnt vmcnt(0)" : "+v"(xr[0]), "+v"(xr[1])::"memory");
    {
      const unsigned sv[4] = {st1[0].x, st1[0].z, st1[1].x, st1[1].z};
      const unsigned xw[8] = {xr[0].x, xr[0].y, xr[0].z, xr[0].w, xr[1].x, xr[1].y, xr[1].z, xr[1].w};
#pragma unroll
      for (int i = 0; i < 4; ++i) {
        const float rs = __builtin_amdgcn_rcpf(hi2f(sv[i])), nm = -lo2f(sv[i]) * rs;
#pragma unroll
        for (int k = 0; k < 2; ++k) { xf[i][k] = up2(xw[2 * i + k]); qf[i][k] = fma2(xf[i][k], f32x2{rs, rs}, f32x2{nm, nm}); pin(qf[i][k]); }
      }
    }
    asm volatile("s_waitcnt lgkmcnt(0)\n\ts_barrier" ::: "memory");
    sfor<0, 2>([&](auto IP) {
      constexpr int ip = decltype(IP)::v;
      unsigned h[4];
#pragma unroll
      for (int k = 0; k < 2; ++k) {
        const char* p = sm + L_Z + (c >> 2) * KF_CP + (256 * w + 128 * ip + 32 * g + 16 * k + 4 * (c & 3)) * 2;
        const v2u z0 = __builtin_bit_cast(v2u, __builtin_amdgcn_ds_read_tr16_b64_v4i16((LAS s16x4*)p));
        const v2u z1 = __builtin_bit_cast(v2u, __builtin_amdgcn_ds_read_tr16_b64_v4i16((LAS s16x4*)(p + 4 * KF_CP)));
        h[2 * k] = pk2(fma2(qf[2 * ip + k][0], up2(z0.x) + up2(z1.x), xf[2 * ip + k][0]));
        h[2 * k + 1] = pk2(fma2(qf[2 * ip + k][1], up2(z0.y) + up2(z1.y), xf[2 * ip + k][1]));
      }
      bs16c<OFF_H + ip * 4 * 36864>(v4u{h[0], h[1], h[2], h[3]}, srd, g * 36864 + c * 16, (unsigned)(w * 8 * 36864 + cg * 256));
    });
    unsigned f_;
    bl4c<OFF_FW>(f_, srd, fwo, 0u);
    asm volatile("s_waitcnt vmcnt(1)" ::: "memory");
    set_flag(srd, OFF_FH + (w * 144 + cg) * 128, tag, lane);
    asm volatile("s_waitcnt vmcnt(1)" : "+v"(f_)::"memory");
    fwv = f_;
  } else {
    const int ri = blockIdx.x - 144;
    unsigned epv;
    blds4c(mksrd(ws, (w == 0 && ri < 16) ? OFF_ST : 0), lane * 0x100000 + (ri >> 3) * 0x80000, 0u, lbase + L_FFN);
    bl4c<OFF_EP>(epv, srd, 0u, 0u);
    const int sl = w >= 4 ? ri : 112 + ri;
    const bool dostat = sl < 128;
    const int srow = (sl >> 1) * 32 + (sl & 1) * 8 + 2 * (w & 3) + (g >> 1) + 16 * (g & 1);
    v2u xr[9];
    {
      const v4u sx_ = mksrd(xg, dostat ? S * ROWB : 0);
      if (!dostat) __builtin_amdgcn_s_sleep(8);
      sfor<0, 9>([&](auto I) { bl8<decltype(I)::v * 128>(xr[decltype(I)::v], sx_, srow * ROWB + c * 8, 0u); });
    }
    const int u = ri * 8 + w;
    const int kb1 = u / 144, bk1 = u % 144, kb2 = u / 18, bk2 = u % 18;
    const v4u srdo = mksrd(ws, ri < 108 ? OFF_ST : 0);
    unsigned lo = lbase + w * 6144;
    const v4u sw1 = mksrd(w1g, ri < 108 ? D * F * 2 : 0);
    unsigned so1 = (kb1 * 96 + (w & 1) * 48) * (F * 2) + (bk1 >> 1) * 128;
    auto ld1 = [&](auto P) { blds16<decltype(P)::v * 8 * F * 2, decltype(P)::v * 1024>(sw1, (lane >> 3) * (F * 2) + (lane & 7) * 16, so1, lo); };
    const v4u sw2 = mksrd(w2g, ri < 108 ? D * F * 2 : 0);
    unsigned so2 = (kb2 * 96 + (w & 1) * 48) * ROWB + (bk2 >> 1) * 128;
    auto ld2 = [&](auto P) { blds16<decltype(P)::v * 8 * ROWB, 49152 + decltype(P)::v * 1024>(sw2, (lane >> 3) * ROWB + (lane & 7) * 16, so2, lo); };
    asm volatile("s_barrier" ::: "memory");
    sfor<0, 3>(ld1);
    asm volatile("s_waitcnt vmcnt(%1)" : "+v"(epv) : "i"(3) : "memory");
    sfor<3, 6>(ld1);
    tag = __builtin_amdgcn_readfirstlane(epv) + 1u;
    {
#pragma unroll
      for (int i = 0; i < 9; ++i) pin(xr[i]);
      float s = 0.f, s2 = 0.f;
#pragma unroll
      for (int i = 0; i < 9; ++i) { s = sum2(xr[i].x, s); s2 = sum2(xr[i].y, s2); }
      const float mean = row16_sum(s + s2) * (1.0f / D);
      f32x2 q = {0.f, 0.f}, q2 = {0.f, 0.f};
      const f32x2 mm = {mean, mean};
#pragma unroll
      for (int i = 0; i < 9; ++i) { f32x2 d0 = up2(xr[i].x) - mm, d1 = up2(xr[i].y) - mm; q += d0 * d0; q2 += d1 * d1; }
      q += q2;
      const float var = rbf(row16_sum(q.x + q.y) * (1.0f / D));
      if (c == 0) *(v2u*)(sm + L_XS + w * 32 + g * 8) = v2u{pk(mean, __builtin_amdgcn_sqrtf(rbf(var + 1e-5f))), tag};
#pragma unroll
      for (int i = 0; i < 9; ++i) *(v2u*)(sm + L_XI + (w >> 2) * 20736 + (c + 16 * i) * 144 + (w & 3) * 32 + g * 8) = xr[i];
    }
    asm volatile("s_waitcnt lgkmcnt(0)\n\ts_barrier" ::: "memory");
#pragma nounroll
    for (int un = 1; un >= (ri < 16 ? 0 : 1); --un) {
      const int ul = un ? ri : 112 + ri;
#pragma unroll
      for (int r = 0; r < 3; ++r) {
        const int id = tid + 512 * r;
        if (r < 2 || w < 2) bs16c<OFF_XT>(*(const v4u*)(sm + L_XI + un * 20736 + (id >> 3) * 144 + (id & 7) * 16), srd, (id >> 3) * 16384 + (id & 7) * 16, (unsigned)(ul * 128));
      }
    }
    sfor<0, 3>(ld2);
    const char* wl = sm + (w >> 1) * 12288 + (4 * g + (c >> 2)) * 128 + (w & 1) * 64 + (c & 3) * 8;
    auto frag = [&](const char* p) {
      v2u a = __builtin_bit_cast(v2u, __builtin_amdgcn_ds_read_tr16_b64_v4i16((LAS s16x4*)p));
      v2u b = __builtin_bit_cast(v2u, __builtin_amdgcn_ds_read_tr16_b64_v4i16((LAS s16x4*)(p + 2048)));
      return v4u{a.x, a.y, b.x, b.y};
    };
    unsigned dst;
    {
      const int l7 = lane & 7, i = l7 >= 3, un = l7 - 3 * i;
      const int r1 = bk1 % 9, own = 2 * (bk1 / 9) + ((r1 + i + 3) >> 3), tau1 = r1 == 4 ? 8 : 2 * (r1 > 4 ? r1 - 5 : r1) + i;
      const unsigned d1 = OFF_W1 + (own * 172 + p1_unit(lane >= 16 ? 18 : 3 * kb1 + un, tau1)) * 1024;
      const int r2 = bk2 % 9, own2 = 2 * (bk2 / 9) + ((r2 + i + 3) >> 3), P = r2 > 4 ? r2 - 5 : r2;
      const int kst = 3 * kb2 + un, s4 = kst / 36, kl = kst - 36 * s4, kp = kprime(kl / 9, kl % 9);
      const int tp = r2 == 4 ? 0 : (P == s4 ? 7 + i : 1 + 2 * ((P - s4 - 1) & 3) + i);
      const unsigned d2 = OFF_W2 + ((s4 * 4 + own2) * 324 + 2 * p2_unit(kp >> 1, tp) + (kp & 1)) * 1024;
      dst = (lane & 8) ? d2 : d1;
      pin(dst);
    }
    asm volatile("s_waitcnt vmcnt(%0)\n\ts_barrier" ::"i"(3) : "memory");
    if (w == 0) bs16c<OFF_ST1>(*(const v4u*)(sm + L_XS + (lane & 15) * 16), srd, lane < 8 ? (ri < 16 ? (112 + ri) * 128 + l16 : 0x80000000u) : lane < 16 ? ri * 128 + l16 - 128 : 0x80000000u, 0u);
    sfor<3, 6>(ld2);
    {
      float cs[2] = {0.f, 0.f};
#pragma unroll
      for (int i = 0; i < 2; ++i) {
#pragma unroll
        for (int un = 0; un < 3; ++un) {
          const unsigned so = __builtin_amdgcn_readlane(dst, 3 * i + un);
          v4u f = frag(wl + un * 4096 + i * 32);
          cs[i] = sum2(f.w, sum2(f.z, sum2(f.y, sum2(f.x, cs[i]))));
          bs16c<64>(f, srdo, l16, so - 64);
        }
        cs[i] += __shfl_xor(cs[i], 16); cs[i] += __shfl_xor(cs[i], 32);
      }
      const float v = g == 0 ? cs[0] : g == 1 ? cs[1] : 0.f;
      const float h1 = rbf(v), h2 = rbf(v - h1);
      const unsigned f0 = __builtin_amdgcn_readlane(dst, 16), f1 = __builtin_amdgcn_readlane(dst, 19);
      unsigned vo = ((g & 1) ? f1 : f0) + (g < 2 ? ((kb1 >> 1) * 16 + c) * 16 + (kb1 & 1) * 8 : (48 + c) * 16 + kb1 * 8);
      vo = (g >= 2 && kb1 >= 2) ? 0x80000000u : vo;
      bs8c<0>(v2u{pk(h1, h2), pk(v - h1 - h2, 0.f)}, srdo, vo, 0u);
    }
    asm volatile("s_waitcnt vmcnt(7)\n\ts_barrier" ::: "memory");
#pragma unroll
    for (int i = 0; i < 2; ++i) {
#pragma unroll
      for (int un = 0; un < 3; ++un) {
        const unsigned so = __builtin_amdgcn_readlane(dst, 8 + 3 * i + un);
        const v4u f = frag(wl + 49152 + un * 4096 + (bk2 % 9 != 4 ? (c & 3) * 8 + i * 8 : i * 32));
        bs16c<64>(f, srdo, l16, so - 64);
      }
      if (i == 0) {
        asm volatile("s_waitcnt vmcnt(3)\n\ts_barrier" ::: "memory");
        if (w == 0) set_flag(srd, OFF_FW + ri * 128, tag, lane);
      }
    }
    unsigned f_;
    bl4c<OFF_FW>(f_, srd, fwo, 0u);
    asm volatile("s_waitcnt vmcnt(0) lgkmcnt(0)\n\ts_barrier" : "+v"(f_)::"memory");
    fwv = f_;
    if (w == 0) set_flag(srd, OFF_FV + ri * 128, tag, lane);
  }

  const int s = bx & 3, jl = bi & 7;
  const int mt16 = 2 * mt8 + (jl >> 2), nt = jl & 3, wm = w & 3, wk = w >> 2;
  unsigned w1src = OFF_W1 + j * 172 * 1024 + w * 1024;
  unsigned w2src = OFF_W2 + (s * 4 + nt) * 324 * 1024 - 172 * 1024 + w * 1024;
  unsigned a1src = OFF_H + (8 * mt8 + w) * 36864;
  unsigned a2src0 = OFF_HID + ((8 * mt16 + 2 * wm + wk) * 144 + 36 * s + wk) * 1024;
  unsigned a2src1 = OFF_HID + ((8 * mt16 + 2 * wm + 1 - wk) * 144 + 36 * s + wk) * 1024;
  const unsigned hdst = OFF_HID + ((16 * mt8 + 2 * w) * 144 + 36 * s) * 1024;
  unsigned hdj = __builtin_amdgcn_readfirstlane(hdst + jl * 1024);
  asm volatile("" : "+s"(hdj));
  unsigned cnt = OFF_FC + (mt8 * 4 + s) * 1024;
  const unsigned inb = OFF_IN + (mt16 * 4 + nt) * 4 * INB;
  unsigned wf = __builtin_amdgcn_readfirstlane(OFF_FP + ((mt16 * 4 + nt) * 8 + w) * 512);
  asm volatile("" : "+s"(wf));
  unsigned ldsw = lbase + w * 1024;
  const int pr = jl >> 1, odd = jl & 1;
  const bool dovar = wk == (jl >> 2);
  const int mfin = 8 * mt16 + 2 * wm + wk;
  const int pcol = 144 * nt + 16 * (nt & 1) + 32 * s;
  const int scol = 144 * nt + ((nt & 1) ? 0 : 128);

  v4u A1r[19][2], A2r[18][2], Bq[LA + 1], q[3];
  v2u hres[3], q1[3];
  unsigned pollv[4], fl;
  f32x4 acc[9][2], acc2[9][2], sum[2], gram[2], xr[7];
  float mean[2];
#pragma unroll
  for (int i = 0; i < 9; ++i) { acc[i][0] = acc[i][1] = acc2[i][0] = acc2[i][1] = f32x4{0.f, 0.f, 0.f, 0.f}; }
  sum[0] = sum[1] = gram[0] = gram[1] = f32x4{0.f, 0.f, 0.f, 0.f};
  const v4u ones = {0x3f803f80u, 0x3f803f80u, 0x3f803f80u, 0x3f803f80u};
  unsigned bl[3] = {lbase + l16, lbase + l16 + 65536, lbase + l16 + 131072};
  pin(bl[0]); pin(bl[1]); pin(bl[2]);

  auto half = [&](f32x4 a) -> v2u { return v2u{relu2(pk(a[0], a[1])), relu2(pk(a[2], a[3]))}; };
  auto doop = [&](auto K, auto AA, auto BB) {
    constexpr int k = decltype(K)::v, a = decltype(AA)::v, b = decltype(BB)::v;
    if constexpr (k == WAITH) {
      if (blockIdx.x >= 144) {
        if (w == 0) wait_flags_v(srd, OFF_FH + mt8 * 144 * 128, lane < 8 ? (lane * 18 + 17) * 128 : 0x80000000u, tag);
        asm volatile("s_barrier" ::: "memory");
      }
      wait_flags<18>(srd, OFF_FH + (mt8 * 144 + w * 18) * 128, tag, lane);
      asm volatile("s_barrier" ::: "memory");
    }
    if constexpr (k == WAITW) {
      if (!__all(fwv == tag)) wait_flags_v(srd, OFF_FW, fwo, tag);
      asm volatile("s_waitcnt lgkmcnt(0)\n\ts_barrier" ::: "memory");
    }
    if constexpr (k == GL) {
      if constexpr (a == 21) w1src = w < 4 ? w1src : w2src;
      blds16c<a * 8192, ring_pos(8 * a) * 1024>(srd, l16, a <= 21 ? w1src : w2src, ldsw);
    }
    if constexpr (k == A1) bl16c<(2 * a + b) * 1024>(A1r[a][b], srd, l16, a1src);
    if constexpr (k == A2) bl16c<a * 2048>(A2r[a][b], srd, l16, b ? a2src1 : a2src0);
    if constexpr (k == HRES) {
      if constexpr (a < 2) bl8c<OFF_H + a * 256>(hres[a], srd, g * 512 + c * 16, (unsigned)((mfin >> 1) * 36864 + pcol * 64 + wk * 8));
      else bl8c<OFF_H>(hres[2], srd, l16, (unsigned)((mfin >> 1) * 36864 + scol * 64 + wk * 8));
    }
    if constexpr (k == STH) {
      if constexpr (a == 2) {
        v2u x = half(acc[8][0]), y = half(acc[8][1]);
        const bool sw = pr & 1;
        bs16c<8 * 1024>(v4u{sw ? y.x : x.x, sw ? y.y : x.y, sw ? x.x : y.x, sw ? x.y : y.y}, srd, l16, hdst + (((pr ^ odd) & 1) * 144 + pr) * 1024);
      } else {
        constexpr int i = a < 2 ? a : a - 1, m = i & 1, p = i >> 1;
        v2u x = half(acc[2 * p][m]), y = half(acc[2 * p + 1][m]);
        bs16c<(m * 144 + kpair(p)) * 1024>(v4u{x.x, x.y, y.x, y.y}, srd, l16, hdj);
      }
    }
    if constexpr (k == VAR) {
      if (dovar) {
#pragma unroll
        for (int m = 0; m < 2; ++m) {
          const int r = c & 3;
          float sq = r == 0 ? gram[m][0] : r == 1 ? gram[m][1] : r == 2 ? gram[m][2] : gram[m][3];
          float var = rbf(fmaxf(sq * (1.0f / D) - mean[m] * mean[m], 0.f));
          if ((c >> 2) == g) *(float*)(sm + L_RS + (wm * 2 + m) * 64 + c * 4) = __builtin_amdgcn_rcpf(rbf(__builtin_amdgcn_sqrtf(rbf(var + 1e-5f))));
        }
      }
    }
    if constexpr (k == POLL) bl4c<a * 32768>(pollv[a], srd, (lane & 7) * 128, cnt);
    if constexpr (k == POLLV) bl4c<OFF_FV>(fl, srd, (lane < 14 ? lane : 0) * 128, (unsigned)(w * 14 * 128));
    if constexpr (k == FLL) bl4c<0>(fl, srd, (lane & 3) * 128, wf);
    if constexpr (k == XR) xr[a] = *(const f32x4*)(sm + L_X + (w ^ 4) * 4096 + xslot(a) * 1024 + l16);
    if constexpr (k == STP) {
      if constexpr (a < 3) {
        f32x4 x = acc2[1 + 2 * a][0] + xr[1 + 2 * a], y = acc2[2 + 2 * a][0] + xr[2 + 2 * a];
        bs16c<(2 - a) * 8192>(v4u{pk(x[0], x[1]), pk(x[2], x[3]), pk(y[0], y[1]), pk(y[2], y[3])}, srd, l16, inb + ((s + 1 + a) & 3) * INB + (2 * wm + wk) * 1024);
      } else {
        acc2[0][0] += xr[0];
        if (wm != s) bs8c<24576>(v2u{pk(acc2[0][0][0], acc2[0][0][1]), pk(acc2[0][0][2], acc2[0][0][3])}, srd, lane * 8,
                                 inb + wm * INB + ((s - wm - 1) & 3) * 1024 + wk * 512);
      }
    }
  };
  auto rdB = [&](auto UU) {
    constexpr int U = decltype(UU)::v;
    constexpr int off = ring_pos(piece_lo(U < NU ? U : 0)) * 1024;
    if constexpr (U == N1) {
#pragma unroll
      for (int i = 0; i < 3; ++i) { bl[i] += wk * 1024; pin(bl[i]); }
    }
    if constexpr (U < NU) Bq[U % (LA + 1)] = *(const LAS v4u*)(size_t)(bl[off >> 16] + (off & 65535));
  };

  auto fetchq = [&]() {
    pin(fl);
    if (!__all(fl == tag)) wait_flags<4>(srd, wf, tag, lane);
    const unsigned myin = inb + s * INB;
    sfor<0, 3>([&](auto I) { bl16c<decltype(I)::v * 8192>(q[decltype(I)::v], srd, l16, myin + (2 * wm + wk) * 1024); });
    sfor<0, 3>([&](auto I) { bl8c<24576 + decltype(I)::v * 1024>(q1[decltype(I)::v], srd, wm == s ? lane * 8 : 0x80000000u, myin + wk * 512); });
  };
  auto run = [&](auto GG) {
  constexpr int G = decltype(GG)::v;
  if constexpr (G == 1) __builtin_amdgcn_s_setprio(1);
  sfor<0, PGS[G].npre>([&](auto I) { constexpr Op o = PGS[G].pre[decltype(I)::v]; doop(IC<o.k>{}, IC<o.a>{}, IC<o.b>{}); });
  sfor<0, NS>([&](auto SS) {
    constexpr int st = decltype(SS)::v;
    auto ops_at = [&](auto PP) {
      constexpr int p = decltype(PP)::v;
      sfor<first_op(G, st, p), first_op(G, st, p + 1)>([&](auto I) { constexpr Op o = PGS[G].ops[st][decltype(I)::v]; doop(IC<o.k>{}, IC<o.a>{}, IC<o.b>{}); });
    };
    if constexpr (st == S_V) { pin(fl); if (!__all(fl == tag)) wait_flags<14>(srd, OFF_FV + w * 14 * 128, tag, lane); }
    if constexpr (st == S_FL) {
      asm volatile("s_waitcnt vmcnt(0)" ::: "memory");
      set_flag(srd, wf + s * 128, tag, lane);
    }
    if constexpr (xbar(st)) asm volatile("s_waitcnt vmcnt(%0) lgkmcnt(0)\n\ts_barrier" ::"i"(PGS[G].wait[st]) : "memory");
    else asm volatile("s_waitcnt vmcnt(%0)\n\ts_barrier" ::"i"(PGS[G].wait[st]) : "memory");
    if constexpr (st == S_V && G == 0) { if (blockIdx.x == 0 && w == 7) set_flag(srd, OFF_EP, tag, lane); }
    sfor<0, 4>([&](auto CC) {
      constexpr int cl = decltype(CC)::v;
      if constexpr (st == s_pub(cl) && G == 0) { if (wm == 0) set_flag(srd, cnt + cl * 32768 + jl * 128, tag, lane); }
      if constexpr (st == s_chk(cl)) {
        pin(pollv[cl]);
        if (!__all(pollv[cl] == tag)) wait_flags<8>(srd, cnt + cl * 32768, tag, lane);
      }
    });
    if constexpr (st == 0) sfor<0, LA>(rdB);
    if constexpr (st == NS - 1) fetchq();
    ops_at(IC<0>{});
    if constexpr (st >= S_G0 && st < S_G0 + 4) {
      if (dovar) {
        sfor<(st - S_G0) * 5, (st == S_G0 + 3 ? 18 : (st - S_G0 + 1) * 5)>([&](auto KK) {
          constexpr int k = decltype(KK)::v;
          gram[0] = mfma(A1r[k][0], A1r[k][0], gram[0]);
          gram[1] = mfma(A1r[k][1], A1r[k][1], gram[1]);
        });
        pin(gram[0]); pin(gram[1]);
      }
    }
    __builtin_amdgcn_sched_barrier(0);
    sfor<0, 9>([&](auto TT) {
      constexpr int i = decltype(TT)::v, U = 9 * st + i, k = u_k(U), t = u_t(U);
      if constexpr (kfirst(U)) {
        v4u& x = st < NS1 ? A1r[k][0] : A2r[k][0];
        v4u& y = st < NS1 ? A1r[k][1] : A2r[k][1];
        asm volatile("s_waitcnt vmcnt(%2)" : "+v"(x), "+v"(y) : "i"(PGS[G].awt[U]) : "memory");
        if constexpr (st < NS1 || k == 4 || k == 5) {
          unsigned a0 = x.z, a1 = x.w, b0 = y.x, b1 = y.y;
          asm volatile("v_swap_b32 %0, %2\n\tv_swap_b32 %1, %3\n\ts_nop 1" : "+v"(a0), "+v"(a1), "+v"(b0), "+v"(b1));
          x.z = a0; x.w = a1; y.x = b0; y.y = b1;
        }
      }
      if constexpr (U == 53) {
#pragma unroll
        for (int m = 0; m < 2; ++m) { mean[m] = sum[m][0] * (1.0f / D); unsigned nm = pk(-mean[m], -mean[m]); A1r[18][m] = v4u{nm, nm, nm, nm}; }
      }
      if constexpr (st < NS1) {
        acc[t][0] = mfma(Bq[U % (LA + 1)], A1r[k][0], acc[t][0]);
        pin(acc[t][0]);
        rdB(IC<U + LA>{});
        __builtin_amdgcn_sched_barrier(0);
        acc[t][1] = mfma(Bq[U % (LA + 1)], A1r[k][1], acc[t][1]);
        pin(acc[t][1]);
        if constexpr (U < 54 && t == 0) { sum[0] = mfma(ones, A1r[k][0], sum[0]); sum[1] = mfma(ones, A1r[k][1], sum[1]); pin(sum[0]); pin(sum[1]); }
      } else {
        acc2[t][0] = mfma(Bq[U % (LA + 1)], A2r[k][0], acc2[t][0]);
        pin(acc2[t][0]);
        rdB(IC<U + LA>{});
        __builtin_amdgcn_sched_barrier(0);
        acc2[t][1] = mfma(Bq[U % (LA + 1)], A2r[k][1], acc2[t][1]);
        pin(acc2[t][1]);
      }
      if constexpr (U > N1 && U < N1 + UF && i > 0 && u_k(U - 1) == 17) *(f32x4*)(sm + L_X + w * 4096 + xslot(u_t(U - 1)) * 1024 + l16) = acc2[u_t(U - 1)][1];
      if constexpr (U > N1 && U < N1 + UF && i == 8 && k == 17) *(f32x4*)(sm + L_X + w * 4096 + xslot(t) * 1024 + l16) = acc2[t][1];
      ops_at(IC<i + 1>{});
      __builtin_amdgcn_sched_barrier(0);
    });
  });
  *(f32x4*)(sm + L_X + w * 4096 + xslot(7) * 1024 + l16) = acc2[7][1];
  *(f32x4*)(sm + L_X + w * 4096 + xslot(8) * 1024 + l16) = acc2[8][1];
  asm volatile("s_waitcnt lgkmcnt(0)\n\ts_barrier" ::: "memory");
  const f32x4 o0 = acc2[7][0] + *(const f32x4*)(sm + L_X + (w ^ 4) * 4096 + xslot(7) * 1024 + l16);
  const f32x4 o1 = acc2[8][0] + *(const f32x4*)(sm + L_X + (w ^ 4) * 4096 + xslot(8) * 1024 + l16);
  const float rs = *(const float*)(sm + L_RS + (2 * wm + wk) * 64 + c * 4);
  asm volatile("s_waitcnt vmcnt(0)" : "+v"(q[0]), "+v"(q[1]), "+v"(q[2]), "+v"(q1[0]), "+v"(q1[1]), "+v"(q1[2]), "+v"(hres[0]), "+v"(hres[1]), "+v"(hres[2])::"memory");
  const f32x2 rr = {rs, rs};
  {
    f32x2 a = (f32x2{o0[0], o0[1]} + up2(q[1].x)) + (up2(q[0].x) + up2(q[2].x));
    f32x2 b = (f32x2{o0[2], o0[3]} + up2(q[1].y)) + (up2(q[0].y) + up2(q[2].y));
    f32x2 cc = (f32x2{o1[0], o1[1]} + up2(q[1].z)) + (up2(q[0].z) + up2(q[2].z));
    f32x2 d = (f32x2{o1[2], o1[3]} + up2(q[1].w)) + (up2(q[0].w) + up2(q[2].w));
    v4u r = {pk2(fma2(a, rr, up2(hres[0].x))), pk2(fma2(b, rr, up2(hres[0].y))), pk2(fma2(cc, rr, up2(hres[1].x))), pk2(fma2(d, rr, up2(hres[1].y)))};
    bs16c<0>(r, mksrd(out, S * ROWB), (mfin * 16 + c) * ROWB + (pcol + 8 * g) * 2, 0u);
  }
  if (wm == s) {
    f32x2 a = (f32x2{acc2[0][0][0], acc2[0][0][1]} + up2(q1[1].x)) + (up2(q1[0].x) + up2(q1[2].x));
    f32x2 b = (f32x2{acc2[0][0][2], acc2[0][0][3]} + up2(q1[1].y)) + (up2(q1[0].y) + up2(q1[2].y));
    v2u r = {pk2(fma2(a, rr, up2(hres[2].x))), pk2(fma2(b, rr, up2(hres[2].y)))};
    bs8c<0>(r, mksrd(out, S * ROWB), (mfin * 16 + c) * ROWB + (scol + 4 * g) * 2, 0u);
  }
  };
  if (wk) run(IC<0>{}); else run(IC<1>{});
}

extern "C" __global__ void mega_end() {}

extern "C" void kernel_launch(void* const* d_in, const int* in_sizes, int n_in, void* d_out, int out_size, void* d_ws, size_t ws_size,
                              hipStream_t stream) {
  (void)hipFuncSetAttribute((const void*)mega, hipFuncAttributeMaxDynamicSharedMemorySize, L_ALL);
  mega<<<256, 512, L_ALL, stream>>>((const char*)d_in[0], (const char*)d_in[1], (const char*)d_in[2], (const char*)d_in[3], (const char*)d_in[4],
                                    (const unsigned*)d_in[5], (const char*)d_in[7], (const char*)d_in[8], (char*)d_ws, (char*)d_out);
}
```

```cpp
#include <hip/hip_runtime.h>
#include <hip/hip_bf16.h>
#include <stdint.h>

typedef unsigned v4u __attribute__((ext_vector_type(4)));
typedef unsigned v2u __attribute__((ext_vector_type(2)));
typedef float f32x4 __attribute__((ext_vector_type(4)));
typedef float f32x2 __attribute__((ext_vector_type(2)));
typedef __bf16 bf16x2 __attribute__((ext_vector_type(2)));
typedef __bf16 bf16x8 __attribute__((ext_vector_type(8)));
typedef short s16x4 __attribute__((ext_vector_type(4)));
typedef short s16x2 __attribute__((ext_vector_type(2)));
#define LAS __attribute__((address_space(3)))
#define DEVI __device__ __forceinline__

static constexpr int S = 2048, D = 576, F = 4608;
static constexpr int ROWB = D * 2;

static constexpr int OFF_EP = 0;
static constexpr int OFF_F0 = 1024;
static constexpr int OFF_FW = OFF_F0 + 144 * 128;
static constexpr int OFF_FV = OFF_FW + 112 * 128;
static constexpr int OFF_FH = OFF_FV + 112 * 128;
static constexpr int OFF_FC = OFF_FH + 1152 * 128;
static constexpr int OFF_FP = OFF_FC + 1024 * 128;
static constexpr int OFF_ST1 = OFF_FP + 2048 * 128;
static constexpr int OFF_KV = OFF_ST1 + 16384;
static constexpr int OFF_XT = OFF_KV + S * D * 2;
static constexpr int OFF_H = OFF_XT + S * D * 2;
static constexpr int OFF_W1 = OFF_H + S * D * 2;
static constexpr int OFF_W2 = OFF_W1 + 32 * 172 * 1024;
static constexpr int OFF_HID = OFF_W2 + D * F * 2;
static constexpr int OFF_IN = OFF_HID + S * F * 2;
static constexpr int INB = 27 * 1024;
static constexpr int OFF_ST = OFF_IN + 256 * INB;

DEVI float lo2f(unsigned u) { return __uint_as_float(u << 16); }
DEVI float hi2f(unsigned u) { return __uint_as_float(u & 0xffff0000u); }
DEVI f32x2 up2(unsigned u) { return f32x2{lo2f(u), hi2f(u)}; }
DEVI unsigned pk2(f32x2 v) { return __builtin_bit_cast(unsigned, __builtin_convertvector(v, bf16x2)); }
DEVI unsigned pk(float a, float b) { return pk2(f32x2{a, b}); }
DEVI f32x2 fma2(f32x2 a, f32x2 b, f32x2 c) { return __builtin_elementwise_fma(a, b, c); }
DEVI float sum2(unsigned u, float c) { const bf16x2 one = {(__bf16)1.0f, (__bf16)1.0f}; return __builtin_amdgcn_fdot2_f32_bf16(__builtin_bit_cast(bf16x2, u), one, c, false); }
DEVI v4u pk8(f32x4 a, f32x4 b) { return v4u{pk(a[0], a[1]), pk(a[2], a[3]), pk(b[0], b[1]), pk(b[2], b[3])}; }
DEVI f32x4 up4(unsigned a, unsigned b) { return f32x4{lo2f(a), hi2f(a), lo2f(b), hi2f(b)}; }
DEVI float rbf(float a) { return lo2f(pk(a, a)); }
DEVI unsigned relu2(unsigned u) { s16x2 z = {0, 0}; return __builtin_bit_cast(unsigned, __builtin_elementwise_max(__builtin_bit_cast(s16x2, u), z)); }
DEVI f32x4 mfma(v4u a, v4u b, f32x4 c) {
  return __builtin_amdgcn_mfma_f32_16x16x32_bf16(__builtin_bit_cast(bf16x8, a), __builtin_bit_cast(bf16x8, b), c, 0, 0, 0);
}
template <int N> struct IC { static constexpr int v = N; };
template <int I, int N, class Fn> DEVI void sfor(Fn&& f) { if constexpr (I < N) { f(IC<I>{}); sfor<I + 1, N>(f); } }
template <int CTRL> DEVI float dpp_add(float v) {
  return v + __builtin_bit_cast(float, __builtin_amdgcn_update_dpp(0, __builtin_bit_cast(int, v), CTRL, 0xf, 0xf, true));
}
DEVI float row16_sum(float v) { v = dpp_add<0xB1>(v); v = dpp_add<0x4E>(v); v = dpp_add<0x141>(v); return dpp_add<0x140>(v); }
DEVI v4u mksrd(const void* p, unsigned bytes) { return v4u{(unsigned)(size_t)p, (unsigned)((size_t)p >> 32) & 0xffffu, bytes, 0x00020000u}; }

#define BUFLD(name, T, text)                                                                                     \
  template <int IMM, class U> DEVI void name(T& d, v4u srd, unsigned voff, U&& soff) {                           \
    soff = __builtin_amdgcn_readfirstlane(soff);                                                                 \
    asm volatile("" : "+s"(soff));                                                                               \
    asm volatile(text : "=v"(d) : "v"(voff), "s"(srd), "s"(soff + IMM) : "memory");                              \
  }
#define BUFST(name, T, text)                                                                                     \
  template <int IMM, class U> DEVI void name(T d, v4u srd, unsigned voff, U&& soff) {                            \
    soff = __builtin_amdgcn_readfirstlane(soff);                                                                 \
    asm volatile("" : "+s"(soff));                                                                               \
    asm volatile(text ::"v"(d), "v"(voff), "s"(srd), "s"(soff + IMM) : "memory");                                \
  }
BUFLD(bl16, v4u, "buffer_load_dwordx4 %0, %1, %2, %3 offen")
BUFLD(bl8, v2u, "buffer_load_dwordx2 %0, %1, %2, %3 offen")
BUFLD(bl16c, v4u, "buffer_load_dwordx4 %0, %1, %2, %3 offen sc1")
BUFLD(bl8c, v2u, "buffer_load_dwordx2 %0, %1, %2, %3 offen sc1")
BUFLD(bl4c, unsigned, "buffer_load_dword %0, %1, %2, %3 offen sc1")
BUFST(bs16c, v4u, "buffer_store_dwordx4 %0, %1, %2, %3 offen sc1\n\ts_nop 1")
BUFST(bs8c, v2u, "buffer_store_dwordx2 %0, %1, %2, %3 offen sc1")
BUFST(bs4c, unsigned, "buffer_store_dword %0, %1, %2, %3 offen sc1")
template <int IMM, int LIMM, class U, class V> DEVI void blds16(v4u srd, unsigned voff, U&& soff, V&& lds) {
  asm volatile("" : "+s"(soff), "+s"(lds));
  asm volatile("s_mov_b32 m0, %3\n\ts_nop 0\n\tbuffer_load_dwordx4 %0, %1, %2 offen lds" ::"v"(voff), "s"(srd), "s"(soff + IMM), "s"(lds + LIMM) : "memory");
}
template <int IMM, int LIMM, class U, class V> DEVI void blds16c(v4u srd, unsigned voff, U&& soff, V&& lds) {
  asm volatile("" : "+s"(soff), "+s"(lds));
  asm volatile("s_mov_b32 m0, %3\n\ts_nop 0\n\tbuffer_load_dwordx4 %0, %1, %2 offen sc1 lds" ::"v"(voff), "s"(srd), "s"(soff + IMM), "s"(lds + LIMM) : "memory");
}
DEVI void blds4c(v4u srd, unsigned voff, unsigned soff, unsigned lds) {
  asm volatile("s_mov_b32 m0, %3\n\ts_nop 0\n\tbuffer_load_dword %0, %1, %2 offen sc1 lds" ::"v"(voff), "s"(srd), "s"(soff), "s"(lds) : "memory");
}
DEVI void blds4(v4u srd, unsigned voff, unsigned lds) {
  asm volatile("s_mov_b32 m0, %2\n\ts_nop 0\n\tbuffer_load_dword %0, %1, 0 offen lds" ::"v"(voff), "s"(srd), "s"(lds) : "memory");
}
template <class T> DEVI void pin(T& x) { asm volatile("" : "+v"(x)); }
DEVI void lds_rd64(v2u& d, unsigned addr, int off) { asm volatile("ds_read_b64 %0, %1 offset:%2" : "=v"(d) : "v"(addr), "i"(off) : "memory"); }
DEVI void lds_rd128(v4u& d, unsigned addr, int off) { asm volatile("ds_read_b128 %0, %1 offset:%2" : "=v"(d) : "v"(addr), "i"(off) : "memory"); }
DEVI void set_flag(v4u srd, unsigned off, unsigned tag, int lane) { bs4c<0>(tag, srd, lane < 32 ? lane * 4 : 0x80000000u, off); }
DEVI void wait_flags_v(v4u srd, unsigned off, unsigned vo, unsigned tag) {
#pragma nounroll
  for (int spin = 0; spin < (1 << 17); ++spin) {
    unsigned v;
    asm volatile("buffer_load_dword %0, %1, %2, %3 offen sc1\n\ts_waitcnt vmcnt(0)" : "=&v"(v) : "v"(vo), "s"(srd), "s"(off) : "memory");
    vo = v == tag ? 0x80000000u : vo;
    if (__all((int)vo < 0)) break;
    __builtin_amdgcn_s_sleep(2);
  }
}
template <int N> DEVI void wait_flags(v4u srd, unsigned off, unsigned tag, int lane) { wait_flags_v(srd, off, lane < N ? lane * 128 : 0x80000000u, tag); }
DEVI void wait_stats(v4u& v, v4u srd, unsigned vo, unsigned off, unsigned tag) {
#pragma nounroll
  for (int spin = 0; spin < (1 << 17) && !__all(v.y == tag && v.w == tag); ++spin)
    asm volatile("buffer_load_dwordx4 %0, %1, %2, %3 offen sc1\n\ts_waitcnt vmcnt(0)" : "=&v"(v) : "v"(vo), "s"(srd), "s"(off) : "memory");
}

namespace sch {
constexpr int UF = 126;
constexpr int KT = 12, KR = 18 - KT;
#define HD __host__ __device__ constexpr
HD int p1_unit(int kl, int tau) { return tau < 2 ? 3 * kl + tau : tau == 8 ? 3 * kl + 2 : 19 + 38 * (tau >> 1) + 2 * kl + (tau & 1); }
HD int p2_unit(int kk, int tp) {
  return tp >= 7 ? UF + 2 * kk + (tp - 7) : kk < KT ? 7 * kk + tp : tp < 3 ? 7 * KT + 3 * (kk - KT) + tp : 7 * KT + ((tp - 1) | 1) * KR + 2 * (kk - KT) + ((tp - 1) & 1);
}
HD int kpair(int p) { return p == 0 ? 0 : 4 + 8 * p; }
HD int kprime(int pr, int r) { return r == 4 ? 8 + pr : kpair(r % 5) + 2 * pr + r / 5; }

constexpr int ring_pos(int f) { return f < 304 ? f % 152 : (f - 304) % 120; }
constexpr int ring_prev(int f) { return f < 424 ? f - 152 : f - 120; }
constexpr int LA = 4, CAP = 4, WST = 2;
constexpr int N1 = 171, NU = N1 + 162, NS1 = 19, NS = 37, NR = 62;
struct UMap { int k[NU], t[NU], km[NU]; };
constexpr UMap mkmap() {
  UMap M{};
  for (int k = 0; k < 19; ++k) for (int t = 0; t < 9; ++t) { M.k[p1_unit(k, t)] = k; M.t[p1_unit(k, t)] = t; }
  for (int k = 0; k < 18; ++k) for (int t = 0; t < 9; ++t) { M.k[N1 + p2_unit(k, t)] = k; M.t[N1 + p2_unit(k, t)] = t; }
  for (int U = 0; U < NU; ++U) { int m = (U == 0 || U == N1) ? -1 : M.km[U - 1]; m = M.k[U] > m ? M.k[U] : m; M.km[U] = m > 17 ? 17 : m; }
  return M;
}
constexpr UMap UM = mkmap();
constexpr int u_k(int U) { return UM.k[U]; }
constexpr int u_t(int U) { return UM.t[U]; }
constexpr int c_sth(int i) { return i < 3 ? 0 : (i - 1) >> 1; }
constexpr int s_sth(int G, int i) {
  int st = 0;
  for (int k = 0; k <= i; ++k) { st += k == 8 ? 0 : 1; int rdy = (57 + 38 * c_sth(k) + 8) / 9 - G; if (st < rdy) st = rdy; }
  return st;
}
constexpr int s_pub(int c) { return s_sth(0, 2 + 2 * c) + 3; }
constexpr int s_chk(int c) { return s_pub(c) + (c == 3 ? 5 : 5); }
constexpr int xdone(int gr) { return N1 + 7 * KT + (3 + 2 * gr) * KR; }
constexpr int s_x(int gr) { return (xdone(gr) - 1) / 9 + 1; }
constexpr int S_FL = s_x(2) + 2;
constexpr int a2class(int i) { return i < 12 ? 0 : (i - 4) >> 3; }
constexpr int piece_lo(int U) { return U < N1 ? U : 172 + 2 * (U - N1); }
constexpr int piece_hi(int U) { return U < N1 ? U : 172 + 2 * (U - N1) + 1; }
constexpr int kmaxu(int U1) { return UM.km[U1]; }
constexpr int kmax(int st) { return kmaxu(9 * st + 8); }
constexpr bool kfirst(int U) { return u_k(U) < 18 && (U == 0 || U == N1 || kmaxu(U) > kmaxu(U - 1)); }
constexpr int need_round(int st) { int U = 9 * st + 8 + LA; return piece_hi(U > NU - 1 ? NU - 1 : U) >> 3; }
constexpr int gl_need(int r) { int st = 0; while (need_round(st) < r) ++st; return st; }
constexpr int a_need(int ph, int i) { int st = ph ? NS1 : 0; while (2 * kmax(st) + 1 < i) ++st; return st; }
enum { GL, A1, A2, STH, POLL, XR, STP, HRES, FLL, VAR, WAITH, POLLV, WAITW, XW };
constexpr int S_V = 8;
constexpr int S_G0 = 7;
struct Op { int k, a, b, pos; };
struct Prog {
  Op pre[40]; int npre;
  Op ops[NS][24]; int nops[NS];
  int wait[NS], awt[NU], xl[NS];
  bool ok;
};
constexpr Prog build(int G) {
  Prog P{};
  P.ok = true;
  int cnt = 0, gl = -1, a1 = -1, a2 = -1;
  int glseq[NR] = {}, a1seq[36] = {}, a2seq[36] = {}, extra[NS + 8] = {};
  {
    int n = 0;
    auto pre = [&](int k, int a, int b, int wgt) { P.pre[n++] = Op{k, a, b, 0}; cnt += wgt; return cnt; };
    pre(WAITW, 0, 0, 0);
    while (gl < 7) { ++gl; glseq[gl] = pre(GL, gl, 0, 1); }
    pre(WAITH, 0, 0, 0);
    while (a1 < 2 * kmax(G) + 1) { ++a1; a1seq[a1] = pre(A1, a1 >> 1, a1 & 1, 1); }
    P.npre = n;
  }
  for (int st = 0; st < NS; ++st) {
    if (gl < need_round(st) || (st < NS1 ? a1 : a2) < 2 * kmax(st) + 1) P.ok = false;
    int need = glseq[need_round(st)];
    need = extra[st] > need ? extra[st] : need;
    P.wait[st] = cnt - need > 63 ? 63 : cnt - need;
    Op ld[24] = {}; int nl = 0, fixed = 0;
    for (int i = 0; i < 9; ++i) if (s_sth(G, i) == st) fixed += WST;
    for (int gr = 0; gr < 3; ++gr) if (st == s_x(gr)) fixed += (gr ? 1 : 2) * WST;
    {
      int g2 = gl, b1 = a1, b2 = a2;
      const int dead = 9 * (st + G) - 133;
      for (int budget = CAP - fixed;; --budget) {
        const int done = st == 0 ? -1 : piece_hi(9 * st - 1);
        const int BIG = 1000;
        int ng = (g2 < NR - 1 && ring_prev(8 * (g2 + 1) + 7) <= done && (g2 + 1 < 21 || st >= S_V)) ? gl_need(g2 + 1) : BIG;
        int na = BIG;
        if (b1 < 35) na = a_need(0, b1 + 1);
        else if (b2 < 35 && st >= s_chk(a2class(b2 + 1)) && b2 + 1 < (dead > 0 ? dead : 0) + 8) na = a_need(1, b2 + 1);
        int m = ng < na ? ng : na;
        if (m == BIG || (budget <= 0 && m - st > (b1 < 35 ? 1 : 2) + G)) break;
        if (na <= ng) { if (b1 < 35) { ++b1; ld[nl++] = Op{A1, b1 >> 1, b1 & 1, 0}; } else { ++b2; ld[nl++] = Op{A2, b2 >> 1, b2 & 1, 0}; } }
        else { ++g2; ld[nl++] = Op{GL, g2, 0, 0}; }
      }
    }
    int n = 0, cmid = 0;
    auto req = [&](int at, int seq) { if (extra[at] < seq) extra[at] = seq; };
    auto opp = [&](int pos, int k, int a, int b, int wgt) { P.ops[st][n++] = Op{k, a, b, pos}; cnt += wgt; return cnt; };
    auto op = [&](int k, int a, int b, int wgt) { return opp(G ? 9 : 0, k, a, b, wgt); };
    auto stp = [&]() { for (int gr = 0; gr < 3; ++gr) if (st == s_x(gr)) { op(STP, gr, 0, 1); if (gr == 0) op(STP, 3, 0, 0); } };
    for (int gr = 0; gr < 3; ++gr) if (st == s_x(gr)) { opp(0, XR, gr, 0, 0); if (gr == 0) opp(0, XR, 3, 0, 0); }
    for (int c = 0; c < 4; ++c) if (st == s_chk(c) - 2) req(s_chk(c), opp(0, POLL, c, 0, 1));
    if (st == S_V - 3) req(S_V - 1, opp(0, POLLV, 0, 0, 1));
    if (st == NS - 2) req(NS - 1, opp(0, FLL, 0, 0, 1));
    if (G) { cmid = cnt; stp(); }
    for (int il = 0; il < nl; ++il) {
      int q = op(ld[il].k, ld[il].a, ld[il].b, 1);
      if (ld[il].k == GL) glseq[++gl] = q; else if (ld[il].k == A1) a1seq[++a1] = q; else a2seq[++a2] = q;
    }
    if (!G) stp();
    for (int h = 0; h < 9; ++h) if (s_sth(G, h) == st) req(s_pub(c_sth(h)), op(STH, h, 0, 1));
    if (st == s_x(2) - 2) { op(HRES, 0, 0, 1); op(HRES, 1, 0, 1); op(HRES, 2, 0, 1); }
    if (st == 13 - G) op(VAR, 0, 0, 0);
    if (!G) cmid = cnt;
    for (int gr = 0; gr < 3; ++gr) if (st == s_x(gr) - 1) {
      int pos = G ? 9 : xdone(gr) - 9 * st + 2; pos = pos > 9 ? 9 : pos;
      if (G) { for (int i = n; i > 0 && P.ops[st][i - 1].pos == 9; --i) { P.ops[st][i] = P.ops[st][i - 1]; P.ops[st][i - 1] = Op{XW, gr, 0, 9}; } if (n == 0 || P.ops[st][n - 1].pos != 9) P.ops[st][n] = Op{XW, gr, 0, 9}; ++n; }
      else opp(pos, XW, gr, 0, 0);
      P.xl[st + 1] = 9 - pos;
    }
    for (int U = 9 * st; U < 9 * st + 9; ++U) if (kfirst(U)) { int d = cmid - (st < NS1 ? a1seq : a2seq)[2 * u_k(U) + 1]; P.awt[U] = d > 63 ? 63 : d; }
    P.nops[st] = n;
    if (n > 24) P.ok = false;
  }
  return P;
}
constexpr Prog PGS[2] = {build(0), build(1)};
static_assert(PGS[0].ok && PGS[1].ok, "schedule: an operand is needed before it is requested");
static_assert(S_FL < NS - 1 && s_x(0) < s_x(1) && s_x(1) < s_x(2), "exchange steps");
constexpr int first_op(int G, int st, int i) { int n = 0; while (n < PGS[G].nops[st] && PGS[G].ops[st][n].pos < i) ++n; return n; }
constexpr bool xbar(int st) { return st == s_x(0) || st == s_x(1) || st == s_x(2); }
}

static_assert(sch::piece_lo(9 * ((sch::xdone(0) - 3) / 9)) > 303, "exchange area still holds ring pieces");
static constexpr int L_X = 120 * 1024;
static constexpr int L_RS = L_X + 32768;
static constexpr int L_FFN = L_RS + 512, L_ALL = L_FFN + 256;

static constexpr int KF_CP = 4160, KF_CH = 4 * KF_CP;
static constexpr int L_KF = 0, L_KV = 4 * KF_CH, L_RED = L_KV + 18 * 1024, L_Z = L_RED;
static constexpr int L_XS = 98304, L_XI = L_XS + 256;
static_assert(L_XI + 2 * 20736 <= L_FFN, "LDS");
static constexpr int L_STG = L_RED, STGP = 1088, STGW = 5 * STGP;
static_assert(L_STG + 8 * STGW <= L_FFN && L_Z + 8 * KF_CP <= L_FFN && 8 * 12288 + 256 <= L_FFN, "LDS");

extern "C" __global__ __launch_bounds__(512, 2) void mega(const char* __restrict__ xg, const char* __restrict__ kg, const char* __restrict__ vg,
                                                       const char* __restrict__ skg, const char* __restrict__ kfg, const unsigned* __restrict__ ssmD,
                                                       const char* __restrict__ w1g, const char* __restrict__ w2g, char* __restrict__ ws,
                                                       char* __restrict__ out) {
  using namespace sch;
  extern __shared__ __attribute__((aligned(16))) char sm[];
  asm volatile("" ::"s"(xg), "s"(kg), "s"(vg), "s"(skg), "s"(kfg), "s"(ssmD), "s"(w1g), "s"(w2g), "s"(ws), "s"(out));
  const int tid = threadIdx.x, lane = tid & 63;
  const int w = __builtin_amdgcn_readfirstlane(tid >> 6);
  const unsigned lbase = (unsigned)(size_t)(LAS char*)sm;
  const int g = lane >> 4, c = lane & 15;
  const unsigned l16 = lane * 16;
  const v4u srd = mksrd(ws, OFF_ST);
  unsigned tag, fwv;
  bool late = false;
  const int bx = blockIdx.x & 7, bi = blockIdx.x >> 3;
  const int mt8 = (bx >> 2) * 4 + (bi >> 3), j = 8 * (bx & 3) + (bi & 7);
  const int fwu = (min(lane, 11) >> 1) * 144 + ((9 * j) >> 1) + (min(lane, 11) & 1) * 4;
  const unsigned fwo = (fwu < 96 ? fwu / 6 : 4 + (fwu >> 3)) * 128;

  if (__builtin_expect(blockIdx.x < 144, 1)) {
    const int cg = 18 * (blockIdx.x & 7) + (blockIdx.x >> 3), sg = cg >> 4, rb = cg & 15, ch = w & 3, eh = w >> 2;
    unsigned epv;
    unsigned stg = lbase + L_STG + w * STGW;
    const char* stgp = sm + L_STG + w * STGW;
    {
      const unsigned vo = (128 * rb + 16 * w - 8 + (lane >> 3)) * ROWB + sg * 128 + (lane & 7) * 16;
      const v4u sk_ = mksrd(kg, S * ROWB), sv_ = mksrd(vg, S * ROWB);
      blds16<0, STGP>(sk_, vo + 8 * ROWB, 0u, stg);
      blds16<0, 2 * STGP>(sk_, vo + 16 * ROWB, 0u, stg);
      blds16<0, 0>(sk_, vo, 0u, stg);
      blds16<0, 3 * STGP>(sv_, vo + 8 * ROWB, 0u, stg);
      blds16<0, 4 * STGP>(sv_, vo + 16 * ROWB, 0u, stg);
    }
    if (w == 7 && blockIdx.x < 96) {
      unsigned lo, hi, len;
      asm volatile("s_getpc_b64 vcc\n\ts_add_u32 %0, vcc_lo, mega@rel32@lo+4\n\ts_addc_u32 %1, vcc_hi, mega@rel32@hi+12\n\ts_mov_b32 %2, mega_end-mega"
                   : "=s"(lo), "=s"(hi), "=s"(len)::"scc", "vcc");
      blds4(v4u{lo, hi & 0xffffu, len > 0x40000u ? 0u : len, 0x00020000u}, ((blockIdx.x >> 3) * 64 + lane) * 128, lbase + L_FFN);
    }
    bl4c<OFF_EP>(epv, srd, 0u, 0u);
    const int cgl = lane >> 2, tcl = (lane >> 1) & 1, cpl = lane & 1;
    v4u skw, kw[2];
    bl16<0>(skw, mksrd(skg, D * 8), (64 * sg + 4 * cgl + 2 * cpl) * 8, 0u);
    const v2u kmid = *(const v2u*)(kfg + (size_t)(4 * cg + ch) * 4096 + 2048);
    const unsigned dpair = ssmD[2 * cg + (ch >> 1)];
    if (tid < 128) *(v4u*)(sm + L_KV + tid * 16) = v4u{0u, 0u, 0u, 0u};
    asm volatile("s_waitcnt vmcnt(0)" : "+v"(skw), "+v"(epv)::"memory");
    const v4u skf = mksrd(kfg, S * D * 2);
    bl16<0>(kw[0], skf, l16, (unsigned)((4 * cg + ch) * 4096 + eh * 2048));
    bl16<1024>(kw[1], skf, l16, (unsigned)((4 * cg + ch) * 4096 + eh * 2048));
    tag = __builtin_amdgcn_readfirstlane(epv) + 1u;
    {
      const char* p = stgp + tcl * STGP + (2 * cgl + cpl) * 4;
      f32x2 kf[11], r[8];
#pragma unroll
      for (int i = 0; i < 11; ++i) kf[i] = up2(*(const unsigned*)(p + ((5 + i) >> 3) * STGP + ((5 + i) & 7) * 128));
      const f32x2 s0 = {lo2f(skw.x), lo2f(skw.z)}, s1 = {hi2f(skw.x), hi2f(skw.z)}, s2 = {lo2f(skw.y), lo2f(skw.w)}, s3 = {hi2f(skw.y), hi2f(skw.w)};
#pragma unroll
      for (int i = 0; i < 8; ++i)
        r[i] = fma2(kf[i + 3], s0, fma2(kf[i + 2], s1, fma2(kf[i + 1], s2, kf[i] * s3))) * up2(*(const unsigned*)(p + 3 * STGP + i * 128));
      unsigned so = OFF_KV + sg * 262144 + (8 * rb + w) * 128;
      unsigned o0[4] = {pk(r[7].x, r[6].x), pk(r[5].x, r[4].x), pk(r[3].x, r[2].x), pk(r[1].x, r[0].x)};
      unsigned o1[4] = {pk(r[7].y, r[6].y), pk(r[5].y, r[4].y), pk(r[3].y, r[2].y), pk(r[1].y, r[0].y)};
#pragma unroll
      for (int i = 0; i < 4; ++i) {
        const int a = o0[i], b = o1[i];
        o0[i] = __builtin_amdgcn_update_dpp(a, b, 0x114, 0xf, 0xa, false);
        o1[i] = __builtin_amdgcn_update_dpp(b, a, 0x104, 0xf, 0x5, false);
      }
      bs16c<0>(v4u{o0[0], o0[1], o0[2], o0[3]}, srd, (lane >> 3) * 32768 + (lane & 7) * 16, so);
      bs16c<16384>(v4u{o1[0], o1[1], o1[2], o1[3]}, srd, (lane >> 3) * 32768 + (lane & 7) * 16, so);
    }
    asm volatile("s_waitcnt vmcnt(2)" : "+v"(kw[0]), "+v"(kw[1])::"memory");
    {
      auto put = [&](v4u a, v2u n, int X) {
        unsigned a01 = __builtin_amdgcn_alignbit(a.y, a.x, 16), a12 = __builtin_amdgcn_alignbit(a.z, a.y, 16), a23 = __builtin_amdgcn_alignbit(a.w, a.z, 16);
        unsigned a3n = __builtin_amdgcn_alignbit(n.x, a.w, 16), ann = __builtin_amdgcn_alignbit(n.y, n.x, 16);
        char* p = sm + L_KF + ch * KF_CH + X * 16;
        *(v4u*)(p) = a;
        *(v4u*)(p + KF_CP) = v4u{a01, a12, a23, a3n};
        *(v4u*)(p + 2 * KF_CP) = v4u{a.y, a.z, a.w, n.x};
        *(v4u*)(p + 3 * KF_CP) = v4u{a12, a23, a3n, ann};
      };
      unsigned f0 = __builtin_amdgcn_readlane(kw[1].x, 0), f1 = __builtin_amdgcn_readlane(kw[1].y, 0);
      v2u n0 = {(unsigned)__builtin_amdgcn_update_dpp((int)f0, (int)kw[0].x, 0x130, 0xf, 0xf, false),
                (unsigned)__builtin_amdgcn_update_dpp((int)f1, (int)kw[0].y, 0x130, 0xf, 0xf, false)};
      put(kw[0], n0, 128 * eh + lane + 2);
      asm volatile("s_waitcnt vmcnt(0)\n\ts_barrier" ::: "memory");
      if (w == 0) set_flag(srd, OFF_F0 + cg * 128, tag, lane);
      v2u n1 = {(unsigned)__builtin_amdgcn_update_dpp((int)kmid.x, (int)kw[1].x, 0x130, 0xf, 0xf, false),
                (unsigned)__builtin_amdgcn_update_dpp((int)kmid.y, (int)kw[1].y, 0x130, 0xf, 0xf, false)};
      put(kw[1], n1, 128 * eh + 64 + lane + 2);
      if (eh == 0) {
        v2u k0 = {(unsigned)__builtin_amdgcn_readlane(kw[0].x, 0), (unsigned)__builtin_amdgcn_readlane(kw[0].y, 0)};
        if (lane < 2) put(v4u{0u, 0u, 0u, 0u}, lane == 1 ? k0 : v2u{0u, 0u}, lane);
      }
    }
    asm volatile("s_waitcnt lgkmcnt(0)\n\ts_barrier" ::: "memory");
    const unsigned abase = lbase + L_KF + ch * KF_CH + ((1 + c) & 3) * KF_CP + (4 * (g >> 1) - 2 * (g & 1) + 2 + ((1 + c) >> 2)) * 8 + eh * 256;
    v2u Al[2][8], Ah[2][8];
    v4u B[2][8];
    unsigned baddr[4];
#pragma unroll
    for (int e = 0; e < 4; ++e) { int x0 = 32 + 2 * c - 2 * (g >> 1) + (g & 1) - 4 * (4 * eh + e); baddr[e] = lbase + L_KV + x0 * 64 + ((ch + (x0 >> 2)) & 3) * 16; }
    auto rdA = [&](auto E, auto I) { constexpr int e = decltype(E)::v, i = decltype(I)::v; lds_rd64(Al[e & 1][i], abase, 64 * e + 512 * i); lds_rd64(Ah[e & 1][i], abase, 64 * e + 512 * i + 8); };
    auto rdB = [&](auto E, auto I) { constexpr int e = decltype(E)::v, i = decltype(I)::v; lds_rd128(B[e & 1][i], baddr[e], 2048 * i); };
    sfor<0, 8>([&](auto I) { rdA(IC<0>{}, I); });
    v4u AD;
    {
      const int jj = 7 + 8 * (g & 1) - c;
      const unsigned dv = (eh == 0 && g < 2 && jj >= 0 && jj < 8) ? ((ch & 1) ? dpair >> 16 : dpair & 0xffffu) << (16 * (jj & 1)) : 0u;
      AD = v4u{(jj >> 1) == 0 ? dv : 0u, (jj >> 1) == 1 ? dv : 0u, (jj >> 1) == 2 ? dv : 0u, (jj >> 1) == 3 ? dv : 0u};
      pin(AD);
    }
    wait_flags<2>(srd, OFF_F0 + (sg * 16 + 2 * w) * 128, tag, lane);
    {
      v4u kv[2];
      unsigned so = OFF_KV + cg * 16384 + w * 2048;
      bl16c<0>(kv[0], srd, l16, so);
      bl16c<1024>(kv[1], srd, l16, so);
      const int tc = 32 * w + 2 * (lane >> 3) + ((lane >> 1) & 1), cc = 2 * (lane & 1) + ((lane >> 2) & 1);
      char* p = sm + L_KV + (tc + 32) * 64 + ((cc + (tc >> 2)) & 3) * 16;
      asm volatile("s_waitcnt vmcnt(0)" : "+v"(kv[0]), "+v"(kv[1])::"memory");
      *(v4u*)p = kv[0];
      *(v4u*)(p + 1024) = kv[1];
    }
    asm volatile("s_waitcnt lgkmcnt(0)\n\ts_barrier"
                 : "+v"(Al[0][0]), "+v"(Al[0][1]), "+v"(Al[0][2]), "+v"(Al[0][3]), "+v"(Al[0][4]), "+v"(Al[0][5]), "+v"(Al[0][6]), "+v"(Al[0][7]),
                   "+v"(Ah[0][0]), "+v"(Ah[0][1]), "+v"(Ah[0][2]), "+v"(Ah[0][3]), "+v"(Ah[0][4]), "+v"(Ah[0][5]), "+v"(Ah[0][6]), "+v"(Ah[0][7])::"memory");
    sfor<0, 8>([&](auto I) { rdB(IC<0>{}, I); });
    f32x4 acc[8];
#pragma unroll
    for (int i = 0; i < 8; ++i) acc[i] = f32x4{0.f, 0.f, 0.f, 0.f};
    v4u xr[2], st1[2];
    f32x2 xf[4][2], qf[4][2];
    auto frA = [&](int s, int a) { return v4u{Al[s][a].x, Al[s][a].y, Ah[s][a].x, Ah[s][a].y}; };
    sfor<0, 4>([&](auto E) {
      constexpr int e = decltype(E)::v, s = e & 1;
      if constexpr (e > 0) {
        asm volatile("s_waitcnt lgkmcnt(0)"
                     : "+v"(Al[s][0]), "+v"(Al[s][1]), "+v"(Al[s][2]), "+v"(Al[s][3]), "+v"(Al[s][4]), "+v"(Al[s][5]), "+v"(Al[s][6]), "+v"(Al[s][7]),
                       "+v"(Ah[s][0]), "+v"(Ah[s][1]), "+v"(Ah[s][2]), "+v"(Ah[s][3]), "+v"(Ah[s][4]), "+v"(Ah[s][5]), "+v"(Ah[s][6]), "+v"(Ah[s][7]),
                       "+v"(B[s][0]), "+v"(B[s][1]), "+v"(B[s][2]), "+v"(B[s][3]), "+v"(B[s][4]), "+v"(B[s][5]), "+v"(B[s][6]), "+v"(B[s][7])::"memory");
      }
      __builtin_amdgcn_s_setprio(3 - e);
      __builtin_amdgcn_sched_barrier(0);
      if constexpr (e < 3) {
        sfor<0, 8>([&](auto BI) {
          constexpr int b = decltype(BI)::v;
          if constexpr (e == 0 && b < 2) asm volatile("s_waitcnt lgkmcnt(%1)" : "+v"(B[s][b]) : "i"(7 - b) : "memory");
          if constexpr (e == 0 && b == 2) asm volatile("s_waitcnt lgkmcnt(0)" : "+v"(B[s][2]), "+v"(B[s][3]), "+v"(B[s][4]), "+v"(B[s][5]), "+v"(B[s][6]), "+v"(B[s][7])::"memory");
          sfor<0, 8 - b>([&](auto AI) {
            constexpr int a = decltype(AI)::v, b0 = e == 0 ? 2 : 0, bb = b - b0;
            acc[a + b] = mfma(frA(s, a), B[s][b], acc[a + b]);
            pin(acc[a + b]);
            constexpr int per = e == 0 ? 3 : 2;
            if constexpr (bb >= 0 && a < 2 * per && per * bb + (a >> 1) < 8 && !(e == 0 && b == 3 && a == 5)) {
              if constexpr (a & 1) rdB(IC<e + 1>{}, IC<per * bb + (a >> 1)>{});
              else rdA(IC<e + 1>{}, IC<per * bb + (a >> 1)>{});
            }
          });
          if constexpr (e == 0) { acc[b] = mfma(AD, B[s][b], acc[b]); pin(acc[b]); if constexpr (b == 3) rdB(IC<1>{}, IC<5>{}); }
          if constexpr (e == 1 && b == 4) {
            bl16c<OFF_ST1>(st1[0], srd, l16, (unsigned)(w * 2048));
            bl16c<OFF_ST1 + 1024>(st1[1], srd, l16, (unsigned)(w * 2048));
          }
          if constexpr (e == 2 && b == 4) {
            asm volatile("s_waitcnt vmcnt(0)" : "+v"(st1[0]), "+v"(st1[1])::"memory");
            wait_stats(st1[0], srd, l16, OFF_ST1 + w * 2048, tag);
            wait_stats(st1[1], srd, l16, OFF_ST1 + w * 2048 + 1024, tag);
            unsigned xs = OFF_XT + cg * 16384 + w * 2048;
            bl16c<0>(xr[0], srd, l16, xs);
            bl16c<1024>(xr[1], srd, l16, xs);
          }
          __builtin_amdgcn_sched_barrier(0);
        });
      } else {
        sfor<0, 9>([&](auto CI) {
          constexpr int ct = decltype(CI)::v;
          if constexpr (ct < 8) sfor<0, ct + 1>([&](auto AI) { constexpr int a = decltype(AI)::v; acc[ct] = mfma(frA(s, a), B[s][ct - a], acc[ct]); pin(acc[ct]); });
          if constexpr (ct > 0) *(v2u*)(sm + L_Z + w * KF_CP + (256 * (ct - 1) + 16 * c + 4 * g) * 2) = v2u{pk(acc[ct - 1][0], acc[ct - 1][1]), pk(acc[ct - 1][2], acc[ct - 1][3])};
          __builtin_amdgcn_sched_barrier(0);
        });
      }
    });
    asm volatile("s_waitcnt vmcnt(0)" : "+v"(xr[0]), "+v"(xr[1])::"memory");
    {
      const unsigned sv[4] = {st1[0].x, st1[0].z, st1[1].x, st1[1].z};
      const unsigned xw[8] = {xr[0].x, xr[0].y, xr[0].z, xr[0].w, xr[1].x, xr[1].y, xr[1].z, xr[1].w};
#pragma unroll
      for (int i = 0; i < 4; ++i) {
        const float rs = __builtin_amdgcn_rcpf(hi2f(sv[i])), nm = -lo2f(sv[i]) * rs;
#pragma unroll
        for (int k = 0; k < 2; ++k) { xf[i][k] = up2(xw[2 * i + k]); qf[i][k] = fma2(xf[i][k], f32x2{rs, rs}, f32x2{nm, nm}); pin(qf[i][k]); }
      }
    }
    asm volatile("s_waitcnt lgkmcnt(0)\n\ts_barrier" ::: "memory");
    sfor<0, 2>([&](auto IP) {
      constexpr int ip = decltype(IP)::v;
      unsigned h[4];
#pragma unroll
      for (int k = 0; k < 2; ++k) {
        const char* p = sm + L_Z + (c >> 2) * KF_CP + (256 * w + 128 * ip + 32 * g + 16 * k + 4 * (c & 3)) * 2;
        const v2u z0 = __builtin_bit_cast(v2u, __builtin_amdgcn_ds_read_tr16_b64_v4i16((LAS s16x4*)p));
        const v2u z1 = __builtin_bit_cast(v2u, __builtin_amdgcn_ds_read_tr16_b64_v4i16((LAS s16x4*)(p + 4 * KF_CP)));
        h[2 * k] = pk2(fma2(qf[2 * ip + k][0], up2(z0.x) + up2(z1.x), xf[2 * ip + k][0]));
        h[2 * k + 1] = pk2(fma2(qf[2 * ip + k][1], up2(z0.y) + up2(z1.y), xf[2 * ip + k][1]));
      }
      bs16c<OFF_H + ip * 4 * 36864>(v4u{h[0], h[1], h[2], h[3]}, srd, g * 36864 + c * 16, (unsigned)(w * 8 * 36864 + cg * 256));
    });
    unsigned f_;
    bl4c<OFF_FW>(f_, srd, fwo, 0u);
    asm volatile("s_waitcnt vmcnt(1)" ::: "memory");
    set_flag(srd, OFF_FH + (w * 144 + cg) * 128, tag, lane);
    asm volatile("s_waitcnt vmcnt(1)" : "+v"(f_)::"memory");
    fwv = f_;
  } else {
    const int ri = blockIdx.x - 144;
    unsigned epv;
    blds4c(mksrd(ws, (w == 0 && ri < 16) ? OFF_ST : 0), lane * 0x100000 + (ri >> 3) * 0x80000, 0u, lbase + L_FFN);
    bl4c<OFF_EP>(epv, srd, 0u, 0u);
    const int sl = w >= 4 ? ri : 112 + ri;
    const bool dostat = sl < 128;
    const int srow = (sl >> 1) * 32 + (sl & 1) * 8 + 2 * (w & 3) + (g >> 1) + 16 * (g & 1);
    v2u xr[9];
    {
      const v4u sx_ = mksrd(xg, dostat ? S * ROWB : 0);
      if (!dostat) __builtin_amdgcn_s_sleep(8);
      sfor<0, 9>([&](auto I) { bl8<decltype(I)::v * 128>(xr[decltype(I)::v], sx_, srow * ROWB + c * 8, 0u); });
    }
    const bool dow = ri >= 16 || w < 6;
    const int u = ri < 16 ? ri * 6 + w : ri * 8 - 32 + w;
    const int kb1 = u / 144, bk1 = u % 144, kb2 = u / 18, bk2 = u % 18;
    const v4u srdo = mksrd(ws, dow ? OFF_ST : 0);
    unsigned lo = lbase + w * 6144;
    const v4u sw1 = mksrd(w1g, dow ? D * F * 2 : 0);
    unsigned so1 = (kb1 * 96 + (w & 1) * 48) * (F * 2) + (bk1 >> 1) * 128;
    auto ld1 = [&](auto P) { blds16<decltype(P)::v * 8 * F * 2, decltype(P)::v * 1024>(sw1, (lane >> 3) * (F * 2) + (lane & 7) * 16, so1, lo); };
    const v4u sw2 = mksrd(w2g, dow ? D * F * 2 : 0);
    unsigned so2 = (kb2 * 96 + (w & 1) * 48) * ROWB + (bk2 >> 1) * 128;
    auto ld2 = [&](auto P) { blds16<decltype(P)::v * 8 * ROWB, 49152 + decltype(P)::v * 1024>(sw2, (lane >> 3) * ROWB + (lane & 7) * 16, so2, lo); };
    asm volatile("s_barrier" ::: "memory");
    sfor<0, 3>(ld1);
    asm volatile("s_waitcnt vmcnt(3)" : "+v"(epv)::"memory");
    sfor<3, 6>(ld1);
    tag = __builtin_amdgcn_readfirstlane(epv) + 1u;
    {
#pragma unroll
      for (int i = 0; i < 9; ++i) pin(xr[i]);
      float s = 0.f, s2 = 0.f;
#pragma unroll
      for (int i = 0; i < 9; ++i) { s = sum2(xr[i].x, s); s2 = sum2(xr[i].y, s2); }
      const float mean = row16_sum(s + s2) * (1.0f / D);
      f32x2 q = {0.f, 0.f}, q2 = {0.f, 0.f};
      const f32x2 mm = {mean, mean};
#pragma unroll
      for (int i = 0; i < 9; ++i) { f32x2 d0 = up2(xr[i].x) - mm, d1 = up2(xr[i].y) - mm; q += d0 * d0; q2 += d1 * d1; }
      q += q2;
      const float var = rbf(row16_sum(q.x + q.y) * (1.0f / D));
      if (c == 0) *(v2u*)(sm + L_XS + w * 32 + g * 8) = v2u{pk(mean, __builtin_amdgcn_sqrtf(rbf(var + 1e-5f))), tag};
#pragma unroll
      for (int i = 0; i < 9; ++i) *(v2u*)(sm + L_XI + (w >> 2) * 20736 + (c + 16 * i) * 144 + (w & 3) * 32 + g * 8) = xr[i];
    }
    asm volatile("s_waitcnt lgkmcnt(0)\n\ts_barrier" ::: "memory");
#pragma nounroll
    for (int un = 1; un >= (ri < 16 ? 0 : 1); --un) {
      const int ul = un ? ri : 112 + ri;
#pragma unroll
      for (int r = 0; r < 3; ++r) {
        const int id = tid + 512 * r;
        if (r < 2 || w < 2) bs16c<OFF_XT>(*(const v4u*)(sm + L_XI + un * 20736 + (id >> 3) * 144 + (id & 7) * 16), srd, (id >> 3) * 16384 + (id & 7) * 16, (unsigned)(ul * 128));
      }
    }
    sfor<0, 3>(ld2);
    const char* wl = sm + (w >> 1) * 12288 + (4 * g + (c >> 2)) * 128 + (w & 1) * 64 + (c & 3) * 8;
    auto frag = [&](const char* p) {
      v2u a = __builtin_bit_cast(v2u, __builtin_amdgcn_ds_read_tr16_b64_v4i16((LAS s16x4*)p));
      v2u b = __builtin_bit_cast(v2u, __builtin_amdgcn_ds_read_tr16_b64_v4i16((LAS s16x4*)(p + 2048)));
      return v4u{a.x, a.y, b.x, b.y};
    };
    unsigned dst;
    {
      const int l7 = lane & 7, i = l7 >= 3, un = l7 - 3 * i;
      const int r1 = bk1 % 9, own = 2 * (bk1 / 9) + ((r1 + i + 3) >> 3), tau1 = r1 == 4 ? 8 : 2 * (r1 > 4 ? r1 - 5 : r1) + i;
      const unsigned d1 = OFF_W1 + (own * 172 + p1_unit(lane >= 16 ? 18 : 3 * kb1 + un, tau1)) * 1024;
      const int r2 = bk2 % 9, own2 = 2 * (bk2 / 9) + ((r2 + i + 3) >> 3), P = r2 > 4 ? r2 - 5 : r2;
      const int kst = 3 * kb2 + un, s4 = kst / 36, kl = kst - 36 * s4, kp = kprime(kl / 9, kl % 9);
      const int tp = r2 == 4 ? 0 : (P == s4 ? 7 + i : 1 + 2 * ((P - s4 - 1) & 3) + i);
      const unsigned d2 = OFF_W2 + ((s4 * 4 + own2) * 324 + 2 * p2_unit(kp >> 1, tp) + (kp & 1)) * 1024;
      dst = (lane & 8) ? d2 : d1;
      pin(dst);
    }
    asm volatile("s_waitcnt vmcnt(3)\n\ts_barrier" ::: "memory");
    if (w == 0) bs16c<OFF_ST1>(*(const v4u*)(sm + L_XS + (lane & 15) * 16), srd, lane < 8 ? (ri < 16 ? (112 + ri) * 128 + l16 : 0x80000000u) : lane < 16 ? ri * 128 + l16 - 128 : 0x80000000u, 0u);
    sfor<3, 6>(ld2);
    {
      float cs[2] = {0.f, 0.f};
#pragma unroll
      for (int i = 0; i < 2; ++i) {
#pragma unroll
        for (int un = 0; un < 3; ++un) {
          const unsigned so = __builtin_amdgcn_readlane(dst, 3 * i + un);
          v4u f = frag(wl + un * 4096 + i * 32);
          cs[i] = sum2(f.w, sum2(f.z, sum2(f.y, sum2(f.x, cs[i]))));
          bs16c<64>(f, srdo, l16, so - 64);
        }
        cs[i] += __shfl_xor(cs[i], 16); cs[i] += __shfl_xor(cs[i], 32);
      }
      const float v = g == 0 ? cs[0] : g == 1 ? cs[1] : 0.f;
      const float h1 = rbf(v), h2 = rbf(v - h1);
      const unsigned f0 = __builtin_amdgcn_readlane(dst, 16), f1 = __builtin_amdgcn_readlane(dst, 19);
      unsigned vo = ((g & 1) ? f1 : f0) + (g < 2 ? ((kb1 >> 1) * 16 + c) * 16 + (kb1 & 1) * 8 : (48 + c) * 16 + kb1 * 8);
      vo = (g >= 2 && kb1 >= 2) ? 0x80000000u : vo;
      bs8c<0>(v2u{pk(h1, h2), pk(v - h1 - h2, 0.f)}, srdo, vo, 0u);
    }
    asm volatile("s_waitcnt vmcnt(7)\n\ts_barrier" ::: "memory");
#pragma unroll
    for (int i = 0; i < 2; ++i) {
#pragma unroll
      for (int un = 0; un < 3; ++un) {
        const unsigned so = __builtin_amdgcn_readlane(dst, 8 + 3 * i + un);
        const v4u f = frag(wl + 49152 + un * 4096 + (bk2 % 9 != 4 ? (c & 3) * 8 + i * 8 : i * 32));
        bs16c<64>(f, srdo, l16, so - 64);
      }
      if (i == 0) {
        asm volatile("s_waitcnt vmcnt(3)\n\ts_barrier" ::: "memory");
        if (w == 0) set_flag(srd, OFF_FW + ri * 128, tag, lane);
      }
    }
    unsigned f_;
    bl4c<OFF_FW>(f_, srd, lane < 56 ? fwo : OFF_FH - OFF_FW + (mt8 * 144 + (lane - 56) * 18 + 17) * 128, 0u);
    asm volatile("s_waitcnt vmcnt(0) lgkmcnt(0)\n\ts_barrier" : "+v"(f_)::"memory");
    fwv = lane < 56 ? f_ : tag;
    late = __all(lane < 56 || f_ == tag);
    if (w == 0) set_flag(srd, OFF_FV + ri * 128, tag, lane);
  }

  const int s = bx & 3, jl = bi & 7;
  const int mt16 = 2 * mt8 + (jl >> 2), nt = jl & 3, wm = w & 3, wk = w >> 2;
  unsigned w1src = OFF_W1 + j * 172 * 1024 + w * 1024;
  unsigned w2src = OFF_W2 + (s * 4 + nt) * 324 * 1024 - 172 * 1024 + w * 1024;
  unsigned a1src = OFF_H + (8 * mt8 + w) * 36864;
  unsigned a2src0 = OFF_HID + ((8 * mt16 + 2 * wm + wk) * 144 + 36 * s + wk) * 1024;
  unsigned a2src1 = OFF_HID + ((8 * mt16 + 2 * wm + 1 - wk) * 144 + 36 * s + wk) * 1024;
  const unsigned hdst = OFF_HID + ((16 * mt8 + 2 * w) * 144 + 36 * s) * 1024;
  unsigned hdj = __builtin_amdgcn_readfirstlane(hdst + jl * 1024);
  asm volatile("" : "+s"(hdj));
  unsigned cnt = OFF_FC + (mt8 * 4 + s) * 1024;
  const unsigned inb = OFF_IN + (mt16 * 4 + nt) * 4 * INB;
  unsigned wf = __builtin_amdgcn_readfirstlane(OFF_FP + ((mt16 * 4 + nt) * 8 + w) * 512);
  asm volatile("" : "+s"(wf));
  unsigned ldsw = lbase + w * 1024;
  const int pr = jl >> 1, odd = jl & 1;
  const bool dovar = wk == (jl >> 2);
  const int mfin = 8 * mt16 + 2 * wm + wk;
  const int pcol = 144 * nt + 16 * (nt & 1) + 32 * s;
  const int scol = 144 * nt + ((nt & 1) ? 0 : 128);

  v4u A1r[19][2], A2r[18][2], Bq[LA + 1], q[3];
  v2u hres[3], q1[3];
  unsigned pollv[4], fl;
  f32x4 acc[9][2], acc2[9][2], sum[2], gram[2];
  v4u xr[3];
  v2u xr1;
  float mean[2];
#pragma unroll
  for (int i = 0; i < 9; ++i) { acc[i][0] = acc[i][1] = acc2[i][0] = acc2[i][1] = f32x4{0.f, 0.f, 0.f, 0.f}; }
  sum[0] = sum[1] = gram[0] = gram[1] = f32x4{0.f, 0.f, 0.f, 0.f};
  const v4u ones = {0x3f803f80u, 0x3f803f80u, 0x3f803f80u, 0x3f803f80u};
  unsigned bl[3] = {lbase + l16, lbase + l16 + 65536, lbase + l16 + 131072};
  pin(bl[0]); pin(bl[1]); pin(bl[2]);

  auto half = [&](f32x4 a) -> v2u { return v2u{relu2(pk(a[0], a[1])), relu2(pk(a[2], a[3]))}; };
  auto doop = [&](auto K, auto AA, auto BB) {
    constexpr int k = decltype(K)::v, a = decltype(AA)::v, b = decltype(BB)::v;
    if constexpr (k == WAITH) {
      if (blockIdx.x >= 144) {
        if (w == 0 && !late) wait_flags_v(srd, OFF_FH + mt8 * 144 * 128, lane < 8 ? (lane * 18 + 17) * 128 : 0x80000000u, tag);
        asm volatile("s_barrier" ::: "memory");
      }
      if (!late) wait_flags<18>(srd, OFF_FH + (mt8 * 144 + w * 18) * 128, tag, lane);
      asm volatile("s_barrier" ::: "memory");
    }
    if constexpr (k == WAITW) {
      if (!__all(fwv == tag)) wait_flags_v(srd, OFF_FW, fwo, tag);
      if (late) wait_flags<18>(srd, OFF_FH + (mt8 * 144 + w * 18) * 128, tag, lane);
      asm volatile("s_waitcnt lgkmcnt(0)\n\ts_barrier" ::: "memory");
    }
    if constexpr (k == GL) {
      if constexpr (a == 21) w1src = w < 4 ? w1src : w2src;
      blds16c<a * 8192, ring_pos(8 * a) * 1024>(srd, l16, a <= 21 ? w1src : w2src, ldsw);
    }
    if constexpr (k == A1) bl16c<(2 * a + b) * 1024>(A1r[a][b], srd, l16, a1src);
    if constexpr (k == A2) bl16c<a * 2048>(A2r[a][b], srd, l16, b ? a2src1 : a2src0);
    if constexpr (k == HRES) {
      if constexpr (a < 2) bl8c<OFF_H + a * 256>(hres[a], srd, g * 512 + c * 16, (unsigned)((mfin >> 1) * 36864 + pcol * 64 + wk * 8));
      else bl8c<OFF_H>(hres[2], srd, l16, (unsigned)((mfin >> 1) * 36864 + scol * 64 + wk * 8));
    }
    if constexpr (k == STH) {
      if constexpr (a == 2) {
        v2u x = half(acc[8][0]), y = half(acc[8][1]);
        const bool sw = pr & 1;
        bs16c<8 * 1024>(v4u{sw ? y.x : x.x, sw ? y.y : x.y, sw ? x.x : y.x, sw ? x.y : y.y}, srd, l16, hdst + (((pr ^ odd) & 1) * 144 + pr) * 1024);
      } else {
        constexpr int i = a < 2 ? a : a - 1, m = i & 1, p = i >> 1;
        v2u x = half(acc[2 * p][m]), y = half(acc[2 * p + 1][m]);
        bs16c<(m * 144 + kpair(p)) * 1024>(v4u{x.x, x.y, y.x, y.y}, srd, l16, hdj);
      }
    }
    if constexpr (k == VAR) {
      if (dovar) {
#pragma unroll
        for (int m = 0; m < 2; ++m) {
          const int r = c & 3;
          float sq = r == 0 ? gram[m][0] : r == 1 ? gram[m][1] : r == 2 ? gram[m][2] : gram[m][3];
          float var = rbf(fmaxf(sq * (1.0f / D) - mean[m] * mean[m], 0.f));
          if ((c >> 2) == g) *(float*)(sm + L_RS + (wm * 2 + m) * 64 + c * 4) = __builtin_amdgcn_rcpf(rbf(__builtin_amdgcn_sqrtf(rbf(var + 1e-5f))));
        }
      }
    }
    if constexpr (k == POLL) bl4c<a * 32768>(pollv[a], srd, (lane & 7) * 128, cnt);
    if constexpr (k == POLLV) bl4c<OFF_FV>(fl, srd, (lane < 14 ? lane : 0) * 128, (unsigned)(w * 14 * 128));
    if constexpr (k == FLL) bl4c<0>(fl, srd, (lane & 3) * 128, wf);
    if constexpr (k == XW) {
      *(v4u*)(sm + L_X + w * 4096 + a * 1024 + l16) = pk8(acc2[1 + 2 * a][1], acc2[2 + 2 * a][1]);
      if constexpr (a == 0) *(v2u*)(sm + L_X + w * 4096 + 3072 + lane * 8) = v2u{pk(acc2[0][1][0], acc2[0][1][1]), pk(acc2[0][1][2], acc2[0][1][3])};
    }
    if constexpr (k == XR) {
      if constexpr (a < 3) xr[a] = *(const v4u*)(sm + L_X + (w ^ 4) * 4096 + a * 1024 + l16);
      else xr1 = *(const v2u*)(sm + L_X + (w ^ 4) * 4096 + 3072 + lane * 8);
    }
    if constexpr (k == STP) {
      if constexpr (a < 3) {
        bs16c<(2 - a) * 8192>(pk8(acc2[1 + 2 * a][0] + up4(xr[a].x, xr[a].y), acc2[2 + 2 * a][0] + up4(xr[a].z, xr[a].w)), srd, l16,
                              inb + ((s + 1 + a) & 3) * INB + (2 * wm + wk) * 1024);
      } else {
        acc2[0][0] += up4(xr1.x, xr1.y);
        if (wm != s) bs8c<24576>(v2u{pk(acc2[0][0][0], acc2[0][0][1]), pk(acc2[0][0][2], acc2[0][0][3])}, srd, lane * 8,
                                 inb + wm * INB + ((s - wm - 1) & 3) * 1024 + wk * 512);
      }
    }
  };
  auto rdB = [&](auto UU) {
    constexpr int U = decltype(UU)::v;
    constexpr int off = ring_pos(piece_lo(U < NU ? U : 0)) * 1024;
    if constexpr (U == N1) {
#pragma unroll
      for (int i = 0; i < 3; ++i) { bl[i] += wk * 1024; pin(bl[i]); }
    }
    if constexpr (U < NU) Bq[U % (LA + 1)] = *(const LAS v4u*)(size_t)(bl[off >> 16] + (off & 65535));
  };

  auto fetchq = [&]() {
    pin(fl);
    if (!__all(fl == tag)) wait_flags<4>(srd, wf, tag, lane);
    const unsigned myin = inb + s * INB;
    sfor<0, 3>([&](auto I) { bl16c<decltype(I)::v * 8192>(q[decltype(I)::v], srd, l16, myin + (2 * wm + wk) * 1024); });
    sfor<0, 3>([&](auto I) { bl8c<24576 + decltype(I)::v * 1024>(q1[decltype(I)::v], srd, wm == s ? lane * 8 : 0x80000000u, myin + wk * 512); });
  };
  auto run = [&](auto GG) {
  constexpr int G = decltype(GG)::v;
  if constexpr (G == 1) __builtin_amdgcn_s_setprio(1);
  sfor<0, PGS[G].npre>([&](auto I) { constexpr Op o = PGS[G].pre[decltype(I)::v]; doop(IC<o.k>{}, IC<o.a>{}, IC<o.b>{}); });
  sfor<0, NS>([&](auto SS) {
    constexpr int st = decltype(SS)::v;
    auto ops_at = [&](auto PP) {
      constexpr int p = decltype(PP)::v;
      sfor<first_op(G, st, p), first_op(G, st, p + 1)>([&](auto I) { constexpr Op o = PGS[G].ops[st][decltype(I)::v]; doop(IC<o.k>{}, IC<o.a>{}, IC<o.b>{}); });
    };
    if constexpr (st == S_V) { pin(fl); if (!__all(fl == tag)) wait_flags<14>(srd, OFF_FV + w * 14 * 128, tag, lane); }
    if constexpr (st == S_FL) {
      asm volatile("s_waitcnt vmcnt(0)" ::: "memory");
      set_flag(srd, wf + s * 128, tag, lane);
    }
    if constexpr (xbar(st)) asm volatile("s_waitcnt vmcnt(%0) lgkmcnt(%1)\n\ts_barrier" ::"i"(PGS[G].wait[st]), "i"(PGS[G].xl[st]) : "memory");
    else asm volatile("s_waitcnt vmcnt(%0)\n\ts_barrier" ::"i"(PGS[G].wait[st]) : "memory");
    if constexpr (st == S_V && G == 0) { if (blockIdx.x == 0 && w == 7) set_flag(srd, OFF_EP, tag, lane); }
    sfor<0, 4>([&](auto CC) {
      constexpr int cl = decltype(CC)::v;
      if constexpr (st == s_pub(cl) && G == 0) { if (wm == 0) set_flag(srd, cnt + cl * 32768 + jl * 128, tag, lane); }
      if constexpr (st == s_chk(cl)) {
        pin(pollv[cl]);
        if (!__all(pollv[cl] == tag)) wait_flags<8>(srd, cnt + cl * 32768, tag, lane);
      }
    });
    if constexpr (st == 0) sfor<0, LA>(rdB);
    if constexpr (st == NS - 1) fetchq();
    ops_at(IC<0>{});
    if constexpr (st >= S_G0 && st < S_G0 + 4) {
      if (dovar) {
        sfor<(st - S_G0) * 5, (st == S_G0 + 3 ? 18 : (st - S_G0 + 1) * 5)>([&](auto KK) {
          constexpr int k = decltype(KK)::v;
          gram[0] = mfma(A1r[k][0], A1r[k][0], gram[0]);
          gram[1] = mfma(A1r[k][1], A1r[k][1], gram[1]);
        });
        pin(gram[0]); pin(gram[1]);
      }
    }
    __builtin_amdgcn_sched_barrier(0);
    sfor<0, 9>([&](auto TT) {
      constexpr int i = decltype(TT)::v, U = 9 * st + i, k = u_k(U), t = u_t(U);
      if constexpr (kfirst(U)) {
        v4u& x = st < NS1 ? A1r[k][0] : A2r[k][0];
        v4u& y = st < NS1 ? A1r[k][1] : A2r[k][1];
        asm volatile("s_waitcnt vmcnt(%2)" : "+v"(x), "+v"(y) : "i"(PGS[G].awt[U]) : "memory");
        if constexpr (st < NS1 || k == 4 || k == 5) {
          unsigned a0 = x.z, a1 = x.w, b0 = y.x, b1 = y.y;
          asm volatile("v_swap_b32 %0, %2\n\tv_swap_b32 %1, %3\n\ts_nop 1" : "+v"(a0), "+v"(a1), "+v"(b0), "+v"(b1));
          x.z = a0; x.w = a1; y.x = b0; y.y = b1;
        }
      }
      if constexpr (U == 53) {
#pragma unroll
        for (int m = 0; m < 2; ++m) { mean[m] = sum[m][0] * (1.0f / D); unsigned nm = pk(-mean[m], -mean[m]); A1r[18][m] = v4u{nm, nm, nm, nm}; }
      }
      if constexpr (st < NS1) {
        acc[t][0] = mfma(Bq[U % (LA + 1)], A1r[k][0], acc[t][0]);
        pin(acc[t][0]);
        rdB(IC<U + LA>{});
        __builtin_amdgcn_sched_barrier(0);
        acc[t][1] = mfma(Bq[U % (LA + 1)], A1r[k][1], acc[t][1]);
        pin(acc[t][1]);
        if constexpr (U < 54 && t == 0) { sum[0] = mfma(ones, A1r[k][0], sum[0]); sum[1] = mfma(ones, A1r[k][1], sum[1]); pin(sum[0]); pin(sum[1]); }
      } else {
        acc2[t][0] = mfma(Bq[U % (LA + 1)], A2r[k][0], acc2[t][0]);
        pin(acc2[t][0]);
        rdB(IC<U + LA>{});
        __builtin_amdgcn_sched_barrier(0);
        acc2[t][1] = mfma(Bq[U % (LA + 1)], A2r[k][1], acc2[t][1]);
        pin(acc2[t][1]);
      }
      ops_at(IC<i + 1>{});
      __builtin_amdgcn_sched_barrier(0);
    });
  });
  *(v4u*)(sm + L_X + w * 4096 + l16) = pk8(acc2[7][1], acc2[8][1]);
  asm volatile("s_waitcnt lgkmcnt(0)\n\ts_barrier" ::: "memory");
  const v4u xo = *(const v4u*)(sm + L_X + (w ^ 4) * 4096 + l16);
  const f32x4 o0 = acc2[7][0] + up4(xo.x, xo.y), o1 = acc2[8][0] + up4(xo.z, xo.w);
  const float rs = *(const float*)(sm + L_RS + (2 * wm + wk) * 64 + c * 4);
  asm volatile("s_waitcnt vmcnt(0)" : "+v"(q[0]), "+v"(q[1]), "+v"(q[2]), "+v"(q1[0]), "+v"(q1[1]), "+v"(q1[2]), "+v"(hres[0]), "+v"(hres[1]), "+v"(hres[2])::"memory");
  const f32x2 rr = {rs, rs};
  {
    f32x2 a = (f32x2{o0[0], o0[1]} + up2(q[1].x)) + (up2(q[0].x) + up2(q[2].x));
    f32x2 b = (f32x2{o0[2], o0[3]} + up2(q[1].y)) + (up2(q[0].y) + up2(q[2].y));
    f32x2 cc = (f32x2{o1[0], o1[1]} + up2(q[1].z)) + (up2(q[0].z) + up2(q[2].z));
    f32x2 d = (f32x2{o1[2], o1[3]} + up2(q[1].w)) + (up2(q[0].w) + up2(q[2].w));
    v4u r = {pk2(fma2(a, rr, up2(hres[0].x))), pk2(fma2(b, rr, up2(hres[0].y))), pk2(fma2(cc, rr, up2(hres[1].x))), pk2(fma2(d, rr, up2(hres[1].y)))};
    bs16c<0>(r, mksrd(out, S * ROWB), (mfin * 16 + c) * ROWB + (pcol + 8 * g) * 2, 0u);
  }
  if (wm == s) {
    f32x2 a = (f32x2{acc2[0][0][0], acc2[0][0][1]} + up2(q1[1].x)) + (up2(q1[0].x) + up2(q1[2].x));
    f32x2 b = (f32x2{acc2[0][0][2], acc2[0][0][3]} + up2(q1[1].y)) + (up2(q1[0].y) + up2(q1[2].y));
    v2u r = {pk2(fma2(a, rr, up2(hres[2].x))), pk2(fma2(b, rr, up2(hres[2].y)))};
    bs8c<0>(r, mksrd(out, S * ROWB), (mfin * 16 + c) * ROWB + (scol + 4 * g) * 2, 0u);
  }
  };
  if (wk) run(IC<0>{}); else run(IC<1>{});
}

extern "C" __global__ void mega_end() {}

extern "C" void kernel_launch(void* const* d_in, const int* in_sizes, int n_in, void* d_out, int out_size, void* d_ws, size_t ws_size,
                              hipStream_t stream) {
  (void)hipFuncSetAttribute((const void*)mega, hipFuncAttributeMaxDynamicSharedMemorySize, L_ALL);
  mega<<<256, 512, L_ALL, stream>>>((const char*)d_in[0], (const char*)d_in[1], (const char*)d_in[2], (const char*)d_in[3], (const char*)d_in[4],
                                    (const unsigned*)d_in[5], (const char*)d_in[7], (const char*)d_in[8], (char*)d_ws, (char*)d_out);
}
```
